```python
import torch

def _softmax(x, axis=-1):
    return torch.softmax(x, dim=axis)
def _gelu(x):
    return torch.nn.functional.gelu(x, approximate="tanh")
def _silu(x):
    return torch.nn.functional.silu(x)
def _sigmoid(x):
    return torch.sigmoid(x)
def _layernorm(x, axis=-1, eps=1e-5):
    m = x.mean(dim=axis, keepdim=True)
    v = x.var(dim=axis, keepdim=True, unbiased=False)
    return (x - m) / torch.sqrt(v + eps)
def _scatter_add(tgt, idx, src, axis=0):
    return tgt.clone().index_add_(axis, idx, src)
def _segment_sum(src, seg, n):
    out = torch.zeros((n,) + src.shape[1:], dtype=src.dtype)
    return out.index_add_(0, seg, src)
def _pad(x, pw):
    flat = tuple(v for pair in reversed(pw) for v in pair)
    return torch.nn.functional.pad(x, flat)

kernel_name = 'sequential_preLN__sliding_local_dilated__dense_activation_gelu__prompt'

def setup_inputs():

    _w = (lambda a: (lambda t: t.to(torch.bfloat16) if t.is_floating_point() else t)(torch.as_tensor(a)))
    def _A_setup():
        B, S, D = 1, 2048, 448
        return {
            "x": _w((lambda *s: torch.randn(*s, dtype=torch.bfloat16) * 0.05)(B, S, D)),
            "wqkv": _w((lambda *s: torch.randn(*s, dtype=torch.bfloat16) * 0.05)(D, 3 * D)),
            "wo": _w((lambda *s: torch.randn(*s, dtype=torch.bfloat16) * 0.05)(D, D)),
        }
    _A_in = _A_setup()

    def _B_setup():
        B, S, D, F = 1, 2048, 448, 1792
        return {
            "x": _w((lambda *s: torch.randn(*s, dtype=torch.bfloat16) * 0.05)(B, S, D)),
            "w1": _w((lambda *s: torch.randn(*s, dtype=torch.bfloat16) * 0.8)(D, F) * (D ** -0.5)),
            "b1": _w((lambda *s: torch.randn(*s, dtype=torch.bfloat16) * 0.8)(F) * 0.01),
            "w2": _w((lambda *s: torch.randn(*s, dtype=torch.bfloat16) * 0.8)(F, D) * (F ** -0.5)),
            "b2": _w((lambda *s: torch.randn(*s, dtype=torch.bfloat16) * 0.8)(D) * 0.01),
        }
    _B_in = _B_setup()

    return {
        "A_x": _A_in["x"],
        "A_wqkv": _A_in["wqkv"],
        "A_wo": _A_in["wo"],
        "B_x": _B_in["x"],
        "B_w1": _B_in["w1"],
        "B_b1": _B_in["b1"],
        "B_w2": _B_in["w2"],
        "B_b2": _B_in["b2"],
    }

def reference(A_x, A_wqkv, A_wo, B_x, B_w1, B_b1, B_w2, B_b2):
    def _A_ref(x, wqkv, wo):
        B, S, D = x.shape
        H = 16; Dh = D // H
        W, dil = 8, 2
        qkv = (lambda x, *d: x.permute(*d))((x @ wqkv).reshape(B, S, 3, H, Dh), 2, 0, 3, 1, 4)
        q, k, v = qkv[0], qkv[1], qkv[2]
        scale = Dh ** -0.5
        pos = torch.arange(S)
        off = torch.arange(W) * dil
        idx = pos[:, None] - off[None, :]
        mask = idx >= 0
        idx_c = torch.where(mask, idx, 0).reshape(-1)
        kg = k[:, :, idx_c, :].reshape(B, H, S, W, Dh)
        vg = v[:, :, idx_c, :].reshape(B, H, S, W, Dh)
        logits = torch.einsum("bhsd,bhswd->bhsw", q, kg) * scale
        logits = torch.where(mask[None, None, :, :], logits, -1e9)
        attn = _softmax(logits, axis=-1)
        o = torch.einsum("bhsw,bhswd->bhsd", attn, vg)
        return (lambda x, *d: x.permute(*d))(o, 0, 2, 1, 3).reshape(B, S, D) @ wo

    def _B_ref(x, w1, b1, w2, b2):
        h = torch.einsum("bsd,df->bsf", x, w1) + b1
        h = _gelu(h)
        return torch.einsum("bsf,fd->bsd", h, w2) + b2

    x = A_x
    h = x + _A_ref(_layernorm(x, axis=-1), A_wqkv, A_wo)
    h2 = h + _B_ref(_layernorm(h, axis=-1), B_w1, B_b1, B_w2, B_b2)
    return h2
```

```cpp
#include <hip/hip_runtime.h>
#include <hip/hip_bf16.h>
#include <stdint.h>

typedef unsigned short u16;
__device__ __forceinline__ float bf2f(u16 v){ return __uint_as_float(((unsigned)v)<<16); }
__device__ __forceinline__ u16 f2bf(float f){ __hip_bfloat16 b = __float2bfloat16(f); return *(u16*)&b; }

constexpr int S=2048, D=448, H=16, DH=28, F=1792;

__global__ void ln_kernel(const u16* x, u16* y){
  int row = blockIdx.x; int t = threadIdx.x;
  float v[7]; float s=0;
  for(int i=0;i<7;i++){ v[i]=bf2f(x[row*D + t + 64*i]); s+=v[i]; }
  for(int m=32;m>=1;m>>=1) s+=__shfl_xor(s,m);
  float mean=s/D; float q=0;
  for(int i=0;i<7;i++){ float d=v[i]-mean; q+=d*d; }
  for(int m=32;m>=1;m>>=1) q+=__shfl_xor(q,m);
  float r=rsqrtf(q/D+1e-5f);
  for(int i=0;i<7;i++) y[row*D+t+64*i]=f2bf((v[i]-mean)*r);
}
__global__ void gemm_kernel(const u16* A, const u16* B, u16* C, const u16* bias, const u16* res, int N, int K, int mode){
  int col = blockIdx.x*64 + threadIdx.x; int row = blockIdx.y;
  if(col>=N) return;
  float acc=0;
  for(int k=0;k<K;k++) acc += bf2f(A[row*K+k])*bf2f(B[k*N+col]);
  if(mode==1){ acc+=bf2f(bias[col]); float u=acc; acc=0.5f*u*(1.f+tanhf(0.7978845608028654f*(u+0.044715f*u*u*u))); }
  if(mode==2){ acc+=bf2f(res[row*N+col]); }
  if(mode==3){ acc+=bf2f(bias[col]); acc+=bf2f(res[row*N+col]); }
  C[row*N+col]=f2bf(acc);
}
__global__ void attn_kernel(const u16* qkv, u16* o){
  int s = blockIdx.x; int h = threadIdx.x; if(h>=H) return;
  const float scale = rsqrtf((float)DH);
  float lg[8]; float mx=-1e30f;
  for(int w=0;w<8;w++){ int j=s-2*w; float a;
    if(j>=0){ a=0; for(int d=0;d<DH;d++) a+=bf2f(qkv[s*3*D+h*DH+d])*bf2f(qkv[j*3*D+D+h*DH+d]); a*=scale; } else a=-1e9f;
    lg[w]=a; mx=fmaxf(mx,a);}
  float sum=0; for(int w=0;w<8;w++){ lg[w]=__expf(lg[w]-mx); sum+=lg[w]; }
  for(int d=0;d<DH;d++){ float a=0; for(int w=0;w<8;w++){ int j=s-2*w; if(j<0) j=0; a+=lg[w]*bf2f(qkv[j*3*D+2*D+h*DH+d]); } o[s*D+h*DH+d]=f2bf(a/sum); }
}

extern "C" void kernel_launch(void* const* d_in, const int* in_sizes, int n_in,
                              void* d_out, int out_size, void* d_ws, size_t ws_size,
                              hipStream_t stream) {
  const u16* x=(const u16*)d_in[0]; const u16* wqkv=(const u16*)d_in[1]; const u16* wo=(const u16*)d_in[2];
  const u16* w1=(const u16*)d_in[4]; const u16* b1=(const u16*)d_in[5]; const u16* w2=(const u16*)d_in[6]; const u16* b2=(const u16*)d_in[7];
  u16* ws=(u16*)d_ws; u16* xn=ws; u16* qkv=xn+S*D; u16* o=qkv+S*3*D; u16* h=o+S*D; u16* hn=h+S*D; u16* g=hn+S*D;
  ln_kernel<<<S,64,0,stream>>>(x,xn);
  gemm_kernel<<<dim3(3*D/64,S),64,0,stream>>>(xn,wqkv,qkv,nullptr,nullptr,3*D,D,0);
  attn_kernel<<<S,64,0,stream>>>(qkv,o);
  gemm_kernel<<<dim3(D/64,S),64,0,stream>>>(o,wo,h,nullptr,x,D,D,2);
  ln_kernel<<<S,64,0,stream>>>(h,hn);
  gemm_kernel<<<dim3(F/64,S),64,0,stream>>>(hn,w1,g,b1,nullptr,F,D,1);
  gemm_kernel<<<dim3(D/64,S),64,0,stream>>>(g,w2,(u16*)d_out,b2,h,D,F,3);
}
```

```cpp
#include <hip/hip_runtime.h>
#include <hip/hip_bf16.h>
#include <stdint.h>
typedef unsigned short u16;
typedef unsigned long long u64;
#define GAS __attribute__((address_space(1)))
__device__ __forceinline__ float bf2f(u16 v){ return __uint_as_float(((unsigned)v)<<16); }
__device__ __forceinline__ u16 f2bf(float f){ __hip_bfloat16 b = __float2bfloat16(f); return *(u16*)&b; }
__device__ __forceinline__ u64 rt() { return __builtin_amdgcn_s_memrealtime(); }
constexpr int S=2048, D=448, H=16, DH=28, F=1792;
#define RLX __ATOMIC_RELAXED, __HIP_MEMORY_SCOPE_AGENT

__device__ void gbar(u64* flags, u64 E, int p){
  asm volatile("s_waitcnt vmcnt(0)":::"memory");
  __syncthreads();
  if(threadIdx.x==0){ __builtin_amdgcn_fence(__ATOMIC_RELEASE,"agent"); asm volatile("s_waitcnt vmcnt(0)":::"memory"); __hip_atomic_store((GAS u64*)(flags+blockIdx.x*16),E+p,RLX); }
  if(threadIdx.x<64){
    for(unsigned sp=0;sp<(1u<<20);sp++){ bool ok=true;
      for(int k=0;k<4;k++){ u64 f=__hip_atomic_load((GAS u64*)(flags+(threadIdx.x*4+k)*16),RLX); ok&= (f-(E+p))<16; }
      if(__all(ok)) break; __builtin_amdgcn_s_sleep(4); }
    __builtin_amdgcn_fence(__ATOMIC_ACQUIRE,"agent"); asm volatile("s_waitcnt vmcnt(0)":::"memory");
  }
  __syncthreads();
}
__device__ void ln_rows(const u16* x, u16* y){
  int lane=threadIdx.x&63; int wv=(blockIdx.x*256+threadIdx.x)>>6;
  for(int row=wv;row<S;row+=1024){
    float v[7]; float s=0;
    for(int i=0;i<7;i++){ v[i]=bf2f(x[row*D + lane + 64*i]); s+=v[i]; }
    for(int m=32;m>=1;m>>=1) s+=__shfl_xor(s,m);
    float mean=s/D; float q=0;
    for(int i=0;i<7;i++){ float d=v[i]-mean; q+=d*d; }
    for(int m=32;m>=1;m>>=1) q+=__shfl_xor(q,m);
    float r=rsqrtf(q/D+1e-5f);
    for(int i=0;i<7;i++) y[row*D+lane+64*i]=f2bf((v[i]-mean)*r);
  }
}
__device__ void gemm(const u16* A, const u16* B, u16* C, const u16* bias, const u16* res, int N, int K, int mode){
  int gt=blockIdx.x*256+threadIdx.x;
  for(int idx=gt; idx<(S/4)*N; idx+=65536){
    int col=idx%N, r0=(idx/N)*4;
    float a0=0,a1=0,a2=0,a3=0;
    for(int k=0;k<K;k++){ float w=bf2f(B[k*N+col]); a0+=bf2f(A[r0*K+k])*w; a1+=bf2f(A[(r0+1)*K+k])*w; a2+=bf2f(A[(r0+2)*K+k])*w; a3+=bf2f(A[(r0+3)*K+k])*w; }
    float acc[4]={a0,a1,a2,a3};
    for(int i=0;i<4;i++){ float a=acc[i]; int row=r0+i;
      if(mode==1){ a+=bf2f(bias[col]); float u=a; a=0.5f*u*(1.f+tanhf(0.7978845608028654f*(u+0.044715f*u*u*u))); }
      if(mode==2){ a+=bf2f(res[row*N+col]); }
      if(mode==3){ a+=bf2f(bias[col]); a+=bf2f(res[row*N+col]); }
      C[row*N+col]=f2bf(a); }
  }
}
__device__ void attn(const u16* qkv, u16* o){
  int gt=blockIdx.x*256+threadIdx.x; if(gt>=S*H) return; int s=gt/H, h=gt%H;
  const float scale = rsqrtf((float)DH);
  float lg[8]; float mx=-1e30f;
  for(int w=0;w<8;w++){ int j=s-2*w; float a;
    if(j>=0){ a=0; for(int d=0;d<DH;d++) a+=bf2f(qkv[s*3*D+h*DH+d])*bf2f(qkv[j*3*D+D+h*DH+d]); a*=scale; } else a=-1e9f;
    lg[w]=a; mx=fmaxf(mx,a);}
  float sum=0; for(int w=0;w<8;w++){ lg[w]=__expf(lg[w]-mx); sum+=lg[w]; }
  for(int d=0;d<DH;d++){ float a=0; for(int w=0;w<8;w++){ int j=s-2*w; if(j<0) j=0; a+=lg[w]*bf2f(qkv[j*3*D+2*D+h*DH+d]); } o[s*D+h*DH+d]=f2bf(a/sum); }
}
__global__ void __launch_bounds__(256,1) mega(const u16* x,const u16* wqkv,const u16* wo,const u16* w1,const u16* b1,const u16* w2,const u16* b2,u16* out,u16* ws,u64* ctl){
  u64 tstart=rt();
  __shared__ u64 sE;
  if(threadIdx.x==0) sE=__hip_atomic_load((GAS u64*)ctl,RLX)+1;
  __syncthreads();
  u64 E=sE; u64* flags=ctl+64;
  u16* xn=ws; u16* qkv=xn+S*D; u16* o=qkv+S*3*D; u16* h=o+S*D; u16* hn=h+S*D; u16* g=hn+S*D;
  ln_rows(x,xn); gbar(flags,E,0);
  if(blockIdx.x==0&&threadIdx.x==0) __hip_atomic_store((GAS u64*)ctl,E+16,RLX);
  gemm(xn,wqkv,qkv,nullptr,nullptr,3*D,D,0); gbar(flags,E,1);
  attn(qkv,o); gbar(flags,E,2);
  gemm(o,wo,h,nullptr,x,D,D,2); gbar(flags,E,3);
  ln_rows(h,hn); gbar(flags,E,4);
  gemm(hn,w1,g,b1,nullptr,F,D,1); gbar(flags,E,5);
  gemm(g,w2,out,b2,h,D,F,3);
}
extern "C" void kernel_launch(void* const* d_in, const int* in_sizes, int n_in,
                              void* d_out, int out_size, void* d_ws, size_t ws_size,
                              hipStream_t stream) {
  const u16* x=(const u16*)d_in[0]; const u16* wqkv=(const u16*)d_in[1]; const u16* wo=(const u16*)d_in[2];
  const u16* w1=(const u16*)d_in[4]; const u16* b1=(const u16*)d_in[5]; const u16* w2=(const u16*)d_in[6]; const u16* b2=(const u16*)d_in[7];
  mega<<<256,256,0,stream>>>(x,wqkv,wo,w1,b1,w2,b2,(u16*)d_out,(u16*)d_ws,(u64*)((char*)d_ws+(64<<20)));
}
```

```cpp
#include <hip/hip_runtime.h>
#include <hip/hip_bf16.h>
#include <stdint.h>

typedef unsigned short u16;
typedef unsigned int u32;
typedef unsigned long long u64;
typedef u32 v4u __attribute__((ext_vector_type(4)));
typedef u32 v2u __attribute__((ext_vector_type(2)));
typedef float f2 __attribute__((ext_vector_type(2)));
typedef float f4 __attribute__((ext_vector_type(4)));
typedef float f16v __attribute__((ext_vector_type(16)));
typedef __bf16 bf8 __attribute__((ext_vector_type(8)));
typedef __bf16 bf2 __attribute__((ext_vector_type(2)));
typedef short s4 __attribute__((ext_vector_type(4)));
typedef short s8 __attribute__((ext_vector_type(8)));
typedef __amdgpu_buffer_rsrc_t rsrc_t;
#define LAS __attribute__((address_space(3)))
#define GAS __attribute__((address_space(1)))
#define DI __device__ __forceinline__

constexpr int S_ = 2048, D_ = 448, F_ = 1792;
constexpr u32 WS_GEN = 0;
constexpr u32 WS_FO = 4096;
constexpr u32 WS_FH = WS_FO + 32 * 4 * 128;
constexpr u32 WS_FG = WS_FH + 32 * 2 * 128;
constexpr u32 WS_ST = 65536;
constexpr u32 WS_O = 262144;
constexpr u32 WS_H = WS_O + 32 * 65536;
constexpr u32 WS_G = WS_H + 32 * 57344;
constexpr u32 WS_STAMP = WS_G + 32 * 229376;
constexpr u32 WS_BYTES = WS_STAMP + 256 * 8 * 32 * 8;
constexpr int SLOT = 28672;
constexpr int L_RING = 0, RING_SLOT = 22528;
constexpr int L_XN = 90112;
constexpr int L_QKV = 114688, QKV_RS = 400;
constexpr int L_WO = 0;
constexpr int L_STA = 57344;
constexpr int L_STB = 0;
constexpr int L_HN = 0;
constexpr int L_RED = 143360;
constexpr int L_CTL = 161792;
constexpr int L_B1 = L_CTL, L_B2 = L_CTL + 1024, L_EP = L_CTL + 1280;
constexpr int LDS_BYTES = 163840;
constexpr u32 SPIN_CAP = 1u << 16;

DI u32 pk2(float lo, float hi) { f2 v = {lo, hi}; return __builtin_bit_cast(u32, __builtin_convertvector(v, bf2)); }
DI float blo(u32 x) { return __uint_as_float(x << 16); }
DI float bhi(u32 x) { return __uint_as_float(x & 0xffff0000u); }
template <int CTRL> DI float dpp(float x) { return __builtin_bit_cast(float, __builtin_amdgcn_mov_dpp(__builtin_bit_cast(int, x), CTRL, 0xf, 0xf, true)); }
DI float sum8(float x) { x += dpp<0xB1>(x); x += dpp<0x4E>(x); x += dpp<0x141>(x); return x; }
DI float xsum16(float x) { auto s = __builtin_amdgcn_permlane16_swap(__float_as_uint(x), __float_as_uint(x), false, false); return __uint_as_float(s[0]) + __uint_as_float(s[1]); }
DI float xsum32(float x) { auto s = __builtin_amdgcn_permlane32_swap(__float_as_uint(x), __float_as_uint(x), false, false); return __uint_as_float(s[0]) + __uint_as_float(s[1]); }
DI float xmax16(float x) { auto s = __builtin_amdgcn_permlane16_swap(__float_as_uint(x), __float_as_uint(x), false, false); return fmaxf(__uint_as_float(s[0]), __uint_as_float(s[1])); }
DI float xmax32(float x) { auto s = __builtin_amdgcn_permlane32_swap(__float_as_uint(x), __float_as_uint(x), false, false); return fmaxf(__uint_as_float(s[0]), __uint_as_float(s[1])); }

DI bf8 trfrag(u32 a0, u32 a1) {
    s4 x = __builtin_amdgcn_ds_read_tr16_b64_v4i16((LAS s4*)(size_t)a0);
    s4 y = __builtin_amdgcn_ds_read_tr16_b64_v4i16((LAS s4*)(size_t)a1);
    s8 r = __builtin_shufflevector(x, y, 0, 1, 2, 3, 4, 5, 6, 7);
    return __builtin_bit_cast(bf8, r);
}
DI bf8 lds128(u32 a) { return __builtin_bit_cast(bf8, *(LAS v4u*)a); }
DI bf8 asbf(v4u x) { return __builtin_bit_cast(bf8, x); }
DI f4 mfma16(bf8 a, bf8 b, f4 c) { return __builtin_amdgcn_mfma_f32_16x16x32_bf16(a, b, c, 0, 0, 0); }
DI f16v mfma32(bf8 a, bf8 b, f16v c) { return __builtin_amdgcn_mfma_f32_32x32x16_bf16(a, b, c, 0, 0, 0); }

#define BAR() asm volatile("s_waitcnt lgkmcnt(0)\n\ts_barrier" ::: "memory")
#define WAITV(n) asm volatile("s_waitcnt vmcnt(" #n ")" ::: "memory")
#define SB() __builtin_amdgcn_sched_barrier(0)

DI void dma16(rsrc_t rs, u32 voff, u32 soff, u32 lds) {
    asm volatile("s_mov_b32 m0, %0\n\ts_nop 0\n\tbuffer_load_dwordx4 %1, %2, %3 offen lds" ::"s"(lds), "v"(voff), "s"(rs), "s"(soff) : "memory");
}
template <int OFF> DI v4u ld16(rsrc_t rs, u32 voff, u32 soff) {
    v4u r; asm volatile("buffer_load_dwordx4 %0, %1, %2, %3 offen offset:%4" : "=v"(r) : "v"(voff), "s"(rs), "s"(soff), "i"(OFF) : "memory"); return r;
}
template <int OFF> DI v4u ld16c(rsrc_t rs, u32 voff, u32 soff) {
    v4u r; asm volatile("buffer_load_dwordx4 %0, %1, %2, %3 offen offset:%4 sc1" : "=v"(r) : "v"(voff), "s"(rs), "s"(soff), "i"(OFF) : "memory"); return r;
}
template <int OFF> DI v2u ld8(rsrc_t rs, u32 voff, u32 soff) {
    v2u r; asm volatile("buffer_load_dwordx2 %0, %1, %2, %3 offen offset:%4" : "=v"(r) : "v"(voff), "s"(rs), "s"(soff), "i"(OFF) : "memory"); return r;
}
template <int OFF> DI v2u ld8c(rsrc_t rs, u32 voff, u32 soff) {
    v2u r; asm volatile("buffer_load_dwordx2 %0, %1, %2, %3 offen offset:%4 sc1" : "=v"(r) : "v"(voff), "s"(rs), "s"(soff), "i"(OFF) : "memory"); return r;
}
DI void st16c(rsrc_t rs, u32 voff, v4u v) { __builtin_amdgcn_raw_buffer_store_b128(v, rs, voff, 0, 16); }
DI void st8c(rsrc_t rs, u32 voff, v2u v) { __builtin_amdgcn_raw_buffer_store_b64(v, rs, voff, 0, 16); }
#define PIN1(a) asm volatile("" : "+v"(a))
#define PIN4(a) asm volatile("" : "+v"((a)[0]), "+v"((a)[1]), "+v"((a)[2]), "+v"((a)[3]))
#define PIN7(a) asm volatile("" : "+v"((a)[0]), "+v"((a)[1]), "+v"((a)[2]), "+v"((a)[3]), "+v"((a)[4]), "+v"((a)[5]), "+v"((a)[6]))

DI void poll_flags(rsrc_t rs, u32 voff, int nflags, u64 ep, int lane) {
    const bool act = lane < nflags;
    const u32 elo = (u32)ep, ehi = (u32)(ep >> 32);
#pragma unroll 1
    for (u32 sp = 0; sp < SPIN_CAP; ++sp) {
        v2u f;
        asm volatile("buffer_load_dwordx2 %0, %1, %2, 0 offen sc1\n\ts_waitcnt vmcnt(0)" : "=&v"(f) : "v"(voff), "s"(rs) : "memory");
        const bool ok = !act || (f.x == elo && f.y == ehi);
        if (__all(ok)) break;
        __builtin_amdgcn_s_sleep(1);
    }
}
DI void put_flag(rsrc_t rs, u32 off, u64 ep, int lane) {
    if (lane == 0) st8c(rs, off, (v2u){(u32)ep, (u32)(ep >> 32)});
}

#define STAMP(k) asm volatile("; MARK " #k)

DI void ln1_group(v4u (&xv)[7], int grp, int lane) {
    float v[56];
#pragma unroll
    for (int c = 0; c < 7; ++c)
#pragma unroll
        for (int e = 0; e < 4; ++e) { v[c * 8 + 2 * e] = blo(xv[c][e]); v[c * 8 + 2 * e + 1] = bhi(xv[c][e]); }
    float s = 0.f;
#pragma unroll
    for (int i = 0; i < 56; ++i) s += v[i];
    const float mean = sum8(s) * (1.0f / 448.0f);
    float q = 0.f;
#pragma unroll
    for (int i = 0; i < 56; ++i) { v[i] -= mean; q += v[i] * v[i]; }
    const float rstd = __builtin_amdgcn_rsqf(sum8(q) * (1.0f / 448.0f) + 1e-5f);
    const int gi = lane & 7, R = 8 * grp + (lane >> 3), tt = R >> 5, h = gi & 1;
    u32 a; int kst;
    if (tt < 2) { a = L_XN + tt * SLOT + (h * 32 + ((R & 31) ^ gi)) * 16 + (gi >> 1) * 1024; kst = 4096; }
    else        { a = L_XN + 2 * SLOT + (h * 16 + ((R & 15) ^ gi)) * 16 + (gi >> 1) * 512; kst = 2048; }
#pragma unroll
    for (int c = 0; c < 7; ++c) {
        v4u o;
#pragma unroll
        for (int e = 0; e < 4; ++e) o[e] = pk2(v[c * 8 + 2 * e] * rstd, v[c * 8 + 2 * e + 1] * rstd);
        *(LAS v4u*)(a + c * kst) = o;
    }
}

DI void qkv_write(const f16v& acc, int m, int row, int hh) {
    const u32 a = L_QKV + row * QKV_RS + m * 64 + hh * 8;
#pragma unroll
    for (int bq = 0; bq < 4; ++bq) {
        v2u o = {pk2(acc[4 * bq], acc[4 * bq + 1]), pk2(acc[4 * bq + 2], acc[4 * bq + 3])};
        if (bq == 3 && hh) o = (v2u){0u, 0u};
        *(LAS v2u*)(a + bq * 16) = o;
    }
}

DI float gelu_tanh(float u) {
    const float k0 = -2.0f * 0.7978845608028654f * 1.4426950408889634f, k1 = k0 * 0.044715f;
    const float z = u * __builtin_fmaf(u * u, k1, k0);
    return u * __builtin_amdgcn_rcpf(1.0f + __builtin_amdgcn_exp2f(z));
}

__global__ void __launch_bounds__(512, 2)
fused_block(const u16* __restrict__ xp, const u16* __restrict__ wqkvp, const u16* __restrict__ wop, const u16* __restrict__ w1p,
            const u16* __restrict__ b1p, const u16* __restrict__ w2p, const u16* __restrict__ b2p, u16* __restrict__ outp, char* __restrict__ wsp) {
    extern __shared__ __attribute__((aligned(16))) char smem[];
    const int tid = threadIdx.x, lane = tid & 63;
    const int w = __builtin_amdgcn_readfirstlane(tid >> 6);
    const int b = blockIdx.x, xi = b >> 3, c = xi >> 3, j = xi & 7, cl = (b & 7) * 4 + c, r0 = cl * 64;
    const rsrc_t rsX = __builtin_amdgcn_make_buffer_rsrc((void*)xp, 0, S_ * D_ * 2, 0x00020000);
    const rsrc_t rsQ = __builtin_amdgcn_make_buffer_rsrc((void*)wqkvp, 0, D_ * 3 * D_ * 2, 0x00020000);
    const rsrc_t rsO = __builtin_amdgcn_make_buffer_rsrc((void*)wop, 0, D_ * D_ * 2, 0x00020000);
    const rsrc_t rs1 = __builtin_amdgcn_make_buffer_rsrc((void*)w1p, 0, D_ * F_ * 2, 0x00020000);
    const rsrc_t rs2 = __builtin_amdgcn_make_buffer_rsrc((void*)w2p, 0, F_ * D_ * 2, 0x00020000);
    const rsrc_t rsW = __builtin_amdgcn_make_buffer_rsrc((void*)wsp, 0, WS_BYTES, 0x00020000);
    STAMP(0);

    v2u gen = {0u, 0u}, b1v = {0u, 0u}, b2v = {0u, 0u};
    if (w == 7) gen = ld8c<0>(rsW, WS_GEN + cl * 128, 0);
    if (w == 6) {
        const rsrc_t rsb1 = __builtin_amdgcn_make_buffer_rsrc((void*)b1p, 0, F_ * 2, 0x00020000);
        const rsrc_t rsb2 = __builtin_amdgcn_make_buffer_rsrc((void*)b2p, 0, D_ * 2, 0x00020000);
        b1v = ld8<0>(rsb1, j * 512 + lane * 8, 0);
        b2v = ld8<0>(rsb2, j * 128 + (lane & 15) * 8, 0);
    }
    v4u xa[7], xb[7];
    {
        const int ra = r0 - 16 + 8 * w + (lane >> 3);
        const u32 va = (u32)(ra < 0 ? 0 : ra) * 896 + (lane & 7) * 16;
        xa[0] = ld16<0>(rsX, va, 0); xa[1] = ld16<128>(rsX, va, 0); xa[2] = ld16<256>(rsX, va, 0); xa[3] = ld16<384>(rsX, va, 0);
        xa[4] = ld16<512>(rsX, va, 0); xa[5] = ld16<640>(rsX, va, 0); xa[6] = ld16<768>(rsX, va, 0);
        if (w < 2) {
            const u32 vb = (u32)(r0 + 48 + 8 * w + (lane >> 3)) * 896 + (lane & 7) * 16;
            xb[0] = ld16<0>(rsX, vb, 0); xb[1] = ld16<128>(rsX, vb, 0); xb[2] = ld16<256>(rsX, vb, 0); xb[3] = ld16<384>(rsX, vb, 0);
            xb[4] = ld16<512>(rsX, vb, 0); xb[5] = ld16<640>(rsX, vb, 0); xb[6] = ld16<768>(rsX, vb, 0);
        }
    }
    u32 vq[3];
#pragma unroll
    for (int i = 0; i < 3; ++i) {
        const int m = 64 * (w + 8 * i) + lane, row = m / 22, gr = m - row * 22;
        const int part = gr < 7 ? 0 : (gr < 14 ? 1 : 2); int sub = gr - 7 * part; sub = sub > 6 ? 6 : sub;
        vq[i] = (u32)(row * 2688 + part * 896 + j * 112 + sub * 16);
    }
    const int k0 = (2 * c + (j & 1)) % 7;
    auto issue_chunk = [&](int i) {
        int kc = k0 + i; kc = kc >= 7 ? kc - 7 : kc;
        const u32 so = (u32)kc * (64 * 2688), ld = L_RING + (i & 3) * RING_SLOT + w * 1024;
        dma16(rsQ, vq[0], so, ld); dma16(rsQ, vq[1], so, ld + 8192);
        if (w < 6) dma16(rsQ, vq[2], so, ld + 16384);
    };
    issue_chunk(0); issue_chunk(1); issue_chunk(2);
    if (w < 6) WAITV(9); else WAITV(6);
    PIN7(xa);
    ln1_group(xa, w, lane);
    if (w < 2) { PIN7(xb); ln1_group(xb, 8 + w, lane); }
    if (w == 7) { PIN1(gen); const u64 e = (((u64)gen.y << 32) | gen.x) + 1; if (lane == 0) *(LAS u64*)(L_EP) = e; }
    if (w == 6) {
        PIN1(b1v); PIN1(b2v);
        *(LAS f4*)(L_B1 + lane * 16) = (f4){blo(b1v.x), bhi(b1v.x), blo(b1v.y), bhi(b1v.y)};
        if (lane < 16) *(LAS f4*)(L_B2 + lane * 16) = (f4){blo(b2v.x), bhi(b2v.x), blo(b2v.y), bhi(b2v.y)};
    }
    STAMP(1);

    const int g4 = lane >> 4, q4 = (lane & 15) >> 2, p4 = lane & 3;
    const int tok32 = lane & 31, hh = lane >> 5;
    {
        const int hA = g4 >> 1, chA = g4 & 1;
        const u32 abase0 = L_RING + (8 * hA + q4) * 352 + chA * 32 + p4 * 8;
        if (w < 6) {
            const u32 abase = abase0 + 56 * w;
            u32 bb[4];
#pragma unroll
            for (int s = 0; s < 4; ++s) bb[s] = L_XN + s * 1024 + (hh * 32 + (tok32 ^ (2 * s + hh))) * 16;
            f16v acc0 = {}, acc1 = {};
#pragma unroll 1
            for (int i = 0; i < 7; ++i) {
                if (i < 5) WAITV(6); else if (i == 5) WAITV(3); else WAITV(0);
                BAR();
                if (i < 4) issue_chunk(i + 3);
                int kc = k0 + i; kc = kc >= 7 ? kc - 7 : kc;
                const u32 aa = abase + (i & 3) * RING_SLOT, bo = kc * 4096;
#pragma unroll
                for (int s = 0; s < 4; ++s) {
                    const bf8 A = trfrag(aa + s * 16 * 352, aa + s * 16 * 352 + 4 * 352);
                    const bf8 B0 = lds128(bb[s] + bo), B1 = lds128(bb[s] + bo + SLOT);
                    acc0 = mfma32(A, B0, acc0); acc1 = mfma32(A, B1, acc1);
                }
            }
            BAR();
            STAMP(2);
            if (j < 7) {
                const u32 vo = (u32)((lane >> 3) * 896 + (((lane & 7) ^ (((lane >> 4) & 3) << 1)) * 16) + j * 128);
#pragma unroll
                for (int i = 0; i < 7; ++i) { int n = w * 7 + i + 14 * c; n = n >= 56 ? n - 56 : n; dma16(rsO, vo, (u32)n * 7168, L_WO + n * 1024); }
            }
            qkv_write(acc0, w, tok32, hh);
            qkv_write(acc1, w, 32 + tok32, hh);
        } else {
            const u32 abase = abase0 + 168 * (w - 6);
            u32 bb[4];
#pragma unroll
            for (int s = 0; s < 4; ++s) bb[s] = L_XN + 2 * SLOT + s * 512 + (hh * 16 + ((lane & 15) ^ (2 * s + hh))) * 16;
            f16v acc0 = {}, acc1 = {}, acc2 = {};
#pragma unroll 1
            for (int i = 0; i < 7; ++i) {
                if (i < 5) WAITV(4); else if (i == 5) WAITV(2); else WAITV(0);
                BAR();
                if (i < 4) issue_chunk(i + 3);
                int kc = k0 + i; kc = kc >= 7 ? kc - 7 : kc;
                const u32 aa = abase + (i & 3) * RING_SLOT, bo = kc * 2048;
#pragma unroll
                for (int s = 0; s < 4; ++s) {
                    const bf8 B = lds128(bb[s] + bo);
                    const u32 a0 = aa + s * 16 * 352;
                    const bf8 A0 = trfrag(a0, a0 + 4 * 352), A1 = trfrag(a0 + 56, a0 + 56 + 4 * 352), A2 = trfrag(a0 + 112, a0 + 112 + 4 * 352);
                    acc0 = mfma32(A0, B, acc0); acc1 = mfma32(A1, B, acc1); acc2 = mfma32(A2, B, acc2);
                }
            }
            BAR();
            STAMP(2);
            if (j < 7) {
                const u32 vo = (u32)((lane >> 3) * 896 + (((lane & 7) ^ (((lane >> 4) & 3) << 1)) * 16) + j * 128);
#pragma unroll
                for (int i = 0; i < 7; ++i) { int n = w * 7 + i + 14 * c; n = n >= 56 ? n - 56 : n; dma16(rsO, vo, (u32)n * 7168, L_WO + n * 1024); }
            }
            if (tok32 < 16) {
                const int m0 = 3 * (w - 6);
                qkv_write(acc0, m0, 64 + tok32, hh); qkv_write(acc1, m0 + 1, 64 + tok32, hh); qkv_write(acc2, m0 + 2, 64 + tok32, hh);
            }
        }
    }
    BAR();
    const u64 ep = *(LAS u64*)(L_EP);
    const int i16 = lane & 15, t4 = w & 3, wh = w >> 2;

    {
        const u32 rowq = L_QKV + (16 + 16 * t4 + i16) * QKV_RS + g4 * 16;
        const bf8 Qf = lds128(rowq + wh * 64);
        const bf8 K0 = lds128(rowq - 16 * QKV_RS + (2 + wh) * 64), K1 = lds128(rowq + (2 + wh) * 64);
        const u32 va = L_QKV + (16 * t4 + 4 * g4 + q4) * QKV_RS + (4 + wh) * 64 + p4 * 8;
        const bf8 V0 = trfrag(va, va + 16 * QKV_RS), V1 = trfrag(va + 32, va + 32 + 16 * QKV_RS);
        const f4 z = {0.f, 0.f, 0.f, 0.f};
        const f4 S0 = mfma16(K0, Qf, z), S1 = mfma16(K1, Qf, z);
        const float cs = 0.18898223650461362f * 1.4426950408889634f;
        float sv[8]; float mx = -3.0e38f;
        const int kbase = r0 - 16 + 16 * t4 + 4 * g4;
#pragma unroll
        for (int e = 0; e < 8; ++e) {
            const int kt = e >> 2, r = e & 3, delta = i16 + 16 - 16 * kt - 4 * g4 - r;
            const bool ok = delta >= 0 && delta <= 14 && !(delta & 1) && (kbase + 16 * kt + r) >= 0;
            sv[e] = ok ? (kt ? S1[r] : S0[r]) * cs : -3.0e38f;
            mx = fmaxf(mx, sv[e]);
        }
        mx = xmax32(xmax16(mx));
        float sum = 0.f;
#pragma unroll
        for (int e = 0; e < 8; ++e) { sv[e] = __builtin_amdgcn_exp2f(sv[e] - mx); sum += sv[e]; }
        const float inv = __builtin_amdgcn_rcpf(xsum32(xsum16(sum)));
        const v4u P = {pk2(sv[0] * inv, sv[1] * inv), pk2(sv[2] * inv, sv[3] * inv), pk2(sv[4] * inv, sv[5] * inv), pk2(sv[6] * inv, sv[7] * inv)};
        const f4 O0 = mfma16(V0, asbf(P), z), O1 = mfma16(V1, asbf(P), z);
        const v4u og = {pk2(O0[0], O0[1]), pk2(O0[2], O0[3]), pk2(O1[0], O1[1]), pk2(O1[2], O1[3])};
        st16c(rsW, WS_O + ((cl * 4 + t4) * 16 + 2 * j + wh) * 1024 + lane * 16, og);
    }
    STAMP(3);
    if (j == 7) {
        WAITV(0);
        put_flag(rsW, WS_FO + (cl * 4 + t4) * 128 + (2 * j + wh) * 8, ep, lane);
        return;
    }
    BAR();

    v2u xr[4];
    if (w < 4) {
        const u32 vx = (u32)(r0 + 16 * t4 + i16) * 896 + j * 128 + g4 * 8;
        xr[0] = ld8<0>(rsX, vx, 0); xr[1] = ld8<32>(rsX, vx, 0); xr[2] = ld8<64>(rsX, vx, 0); xr[3] = ld8<96>(rsX, vx, 0);
    }
    const u32 vw1 = (u32)((lane >> 3) * 3584 + (((lane & 7) ^ (((lane >> 4) & 1) << 2)) * 16) + j * 512);
    auto w1_piece = [&](int n, int kb0, int lbase) {
        dma16(rs1, vw1, (u32)((32 * (kb0 + (n >> 4)) + 8 * (n & 3)) * 3584 + ((n >> 2) & 3) * 128), (u32)(lbase + n * 1024));
    };
#pragma unroll
    for (int i = 0; i < 12; ++i) { int n = w + 8 * i + 24 * c; n = n >= 96 ? n - 96 : n; w1_piece(n, 0, L_STA); }
    if (w < 4) WAITV(16); else WAITV(12);
    put_flag(rsW, WS_FO + (cl * 4 + t4) * 128 + (2 * j + wh) * 8, ep, lane);
    v4u of[16];
    if (w < 4) {
        poll_flags(rsW, WS_FO + (cl * 4 + t4) * 128 + (lane & 15) * 8, 16, ep, lane);
        if (j == 0 && w == 0 && lane == 0) st8c(rsW, WS_GEN + cl * 128, (v2u){(u32)ep, (u32)(ep >> 32)});
        const u32 vo = WS_O + (cl * 4 + t4) * 16384 + lane * 16;
        of[0] = ld16c<0>(rsW, vo, 0); of[1] = ld16c<1024>(rsW, vo, 0); of[2] = ld16c<2048>(rsW, vo, 0); of[3] = ld16c<3072>(rsW, vo, 0);
        of[4] = ld16c<0>(rsW, vo, 4096); of[5] = ld16c<1024>(rsW, vo, 4096); of[6] = ld16c<2048>(rsW, vo, 4096); of[7] = ld16c<3072>(rsW, vo, 4096);
        of[8] = ld16c<0>(rsW, vo, 8192); of[9] = ld16c<1024>(rsW, vo, 8192); of[10] = ld16c<2048>(rsW, vo, 8192); of[11] = ld16c<3072>(rsW, vo, 8192);
        of[12] = ld16c<0>(rsW, vo, 12288); of[13] = ld16c<1024>(rsW, vo, 12288); of[14] = ld16c<2048>(rsW, vo, 12288); of[15] = ld16c<3072>(rsW, vo, 12288);
    } else {
        WAITV(0);
    }
    STAMP(4);
    BAR();
    bf8 A1[28];
    const u32 a1lane = (u32)((w >> 1) * 4096 + (4 * (g4 >> 1) + q4) * 128 + (((4 * (w & 1) + 2 * (g4 & 1) + (p4 >> 1)) ^ (((q4 >> 1) & 1) << 2)) * 16) + (p4 & 1) * 8);
    {
        const u32 a = L_STA + a1lane;
#pragma unroll
        for (int s = 0; s < 12; ++s) {
            const u32 o = (s >> 1) * 16384 + (s & 1) * 1024;
            A1[s] = trfrag(a + o, a + o + 2048);
        }
    }
    v2u hp[4];
    if (w < 4) {
        u32 ao[2][4];
        const int rr0 = 4 * g4 + q4, rr1 = (g4 < 3 ? 16 + 4 * g4 : 24) + q4;
#pragma unroll
        for (int ct = 0; ct < 4; ++ct) {
            ao[0][ct] = L_WO + rr0 * 128 + (((ct ^ ((rr0 >> 1) & 3)) << 1) | (p4 >> 1)) * 16 + (p4 & 1) * 8;
            ao[1][ct] = L_WO + rr1 * 128 + (((ct ^ ((rr1 >> 1) & 3)) << 1) | (p4 >> 1)) * 16 + (p4 & 1) * 8;
        }
        f4 acc[4] = {};
        WAITV(0);
        PIN4(&of[0]); PIN4(&of[4]); PIN4(&of[8]); PIN4(&of[12]); PIN4(xr);
#pragma unroll
        for (int H = 0; H < 16; ++H) {
#pragma unroll
            for (int ct = 0; ct < 4; ++ct) {
                const int cx = ct ^ ((H & 1) << 1);
                const bf8 A = trfrag(ao[0][cx] + H * 3584, ao[1][cx] + H * 3584);
                acc[ct] = mfma16(A, asbf(of[H]), acc[ct]);
            }
        }
        BAR();
        STAMP(5);
        float hv[16]; float s = 0.f;
#pragma unroll
        for (int ct = 0; ct < 4; ++ct) {
            hp[ct] = (v2u){pk2(acc[ct][0] + blo(xr[ct].x), acc[ct][1] + bhi(xr[ct].x)), pk2(acc[ct][2] + blo(xr[ct].y), acc[ct][3] + bhi(xr[ct].y))};
            hv[4 * ct] = blo(hp[ct].x); hv[4 * ct + 1] = bhi(hp[ct].x); hv[4 * ct + 2] = blo(hp[ct].y); hv[4 * ct + 3] = bhi(hp[ct].y);
        }
        const u32 vh = WS_H + ((cl * 2 + (t4 >> 1)) * 28 + 4 * j + (g4 >> 1)) * 1024 + ((g4 & 1) * 32 + 16 * (t4 & 1) + i16) * 16;
        st16c(rsW, vh, (v4u){hp[0].x, hp[0].y, hp[1].x, hp[1].y});
        st16c(rsW, vh + 2048, (v4u){hp[2].x, hp[2].y, hp[3].x, hp[3].y});
#pragma unroll
        for (int e = 0; e < 16; ++e) s += hv[e];
        const float m64 = xsum32(xsum16(s)) * (1.0f / 64.0f);
        float q = 0.f;
#pragma unroll
        for (int e = 0; e < 16; ++e) { const float d = hv[e] - m64; q += d * d; }
        q = xsum32(xsum16(q));
        if (g4 == 0) st8c(rsW, WS_ST + (cl * 8 + j) * 512 + (16 * t4 + i16) * 8, (v2u){__float_as_uint(m64), __float_as_uint(q)});
        WAITV(0);
        put_flag(rsW, WS_FH + (cl * 2 + (t4 >> 1)) * 128 + (2 * j + (t4 & 1)) * 8, ep, lane);
    } else {
        hp[0] = hp[1] = hp[2] = hp[3] = (v2u){0u, 0u};
        BAR();
        STAMP(5);
#pragma unroll
        for (int i = 0; i < 32; ++i) { int n = (w - 4) + 4 * i + 32 * c; n = n >= 128 ? n - 128 : n; w1_piece(n, 6, L_STB); }
    }

    {
        const int tp = wh, u4 = t4;
        poll_flags(rsW, WS_FH + (cl * 2 + tp) * 128 + (lane & 15) * 8, 14, ep, lane);
        v4u hf[7]; v2u st[4];
        const u32 vh = WS_H + ((cl * 2 + tp) * 28 + 7 * u4) * 1024 + lane * 16;
        hf[0] = ld16c<0>(rsW, vh, 0); hf[1] = ld16c<1024>(rsW, vh, 0); hf[2] = ld16c<2048>(rsW, vh, 0); hf[3] = ld16c<3072>(rsW, vh, 0);
        hf[4] = ld16c<0>(rsW, vh, 4096); hf[5] = ld16c<1024>(rsW, vh, 4096); hf[6] = ld16c<2048>(rsW, vh, 4096);
        const u32 vs = WS_ST + (cl * 8 + 4 * hh) * 512 + (32 * tp + tok32) * 8;
        st[0] = ld8c<0>(rsW, vs, 0); st[1] = ld8c<512>(rsW, vs, 0); st[2] = ld8c<1024>(rsW, vs, 0); st[3] = ld8c<1536>(rsW, vs, 0);
        STAMP(6);
        BAR();
        {
            const u32 a = L_STB + a1lane, a2 = a + 65536;
#pragma unroll
            for (int s = 12; s < 28; ++s) {
                const int kb = (s >> 1) - 6;
                const u32 o = (kb & 3) * 16384 + (s & 1) * 1024;
                A1[s] = kb < 4 ? trfrag(a + o, a + o + 2048) : trfrag(a2 + o, a2 + o + 2048);
            }
        }
        WAITV(0);
        PIN7(hf); PIN4(st);
        const bool v3 = hh == 0;
        float m[4], m2[4];
#pragma unroll
        for (int e = 0; e < 4; ++e) { m[e] = __uint_as_float(st[e].x); m2[e] = __uint_as_float(st[e].y); }
        const float mean = xsum32(m[0] + m[1] + m[2] + (v3 ? m[3] : 0.f)) * (1.0f / 7.0f);
        float t = m2[0] + m2[1] + m2[2] + 64.0f * ((m[0] - mean) * (m[0] - mean) + (m[1] - mean) * (m[1] - mean) + (m[2] - mean) * (m[2] - mean));
        if (v3) t += m2[3] + 64.0f * (m[3] - mean) * (m[3] - mean);
        const float rstd = __builtin_amdgcn_rsqf(xsum32(t) * (1.0f / 448.0f) + 1e-5f);
        const float nb = -mean * rstd;
#pragma unroll
        for (int f = 0; f < 7; ++f)
#pragma unroll
            for (int e = 0; e < 4; ++e) hf[f][e] = pk2(__builtin_fmaf(blo(hf[f][e]), rstd, nb), __builtin_fmaf(bhi(hf[f][e]), rstd, nb));
        BAR();
        const u32 ah = L_HN + (tp * 28 + 7 * u4) * 1024 + lane * 16;
#pragma unroll
        for (int f = 0; f < 7; ++f) *(LAS v4u*)(ah + f * 1024) = hf[f];
    }
    const u32 vw2 = (u32)((lane >> 3) * 896 + (((lane & 7) ^ (((lane >> 4) & 3) << 1)) * 16) + j * 128);
    auto w2_piece = [&](int phi, int n, int slot) {
        dma16(rs2, vw2, (u32)((256 * (n >> 2) + 32 * phi + 8 * (n & 3)) * 896), (u32)(slot * SLOT + n * 1024));
    };
    auto w2_slot = [&](int phi, int slot) {
#pragma unroll
        for (int i = 0; i < 4; ++i) { const int n0 = w + 8 * i; if (n0 < 28) { int n = n0 + 7 * c; n = n >= 28 ? n - 28 : n; w2_piece(phi, n, slot); } }
    };
    w2_slot(0, 2); w2_slot(1, 3); w2_slot(2, 4);
    BAR();
    STAMP(7);

    {
        f16v acc0, acc1;
#pragma unroll
        for (int bq = 0; bq < 4; ++bq) {
            const f4 bv = *(LAS f4*)(L_B1 + (32 * w + 8 * bq + 4 * hh) * 4);
#pragma unroll
            for (int r = 0; r < 4; ++r) acc0[4 * bq + r] = bv[r];
        }
        acc1 = acc0;
        if (w < 4) __builtin_amdgcn_s_setprio(1);
        const u32 ab = L_HN + lane * 16;
        bf8 Bq[3][2];
#pragma unroll
        for (int s = 0; s < 2; ++s) { Bq[s][0] = lds128(ab + s * 1024); Bq[s][1] = lds128(ab + s * 1024 + SLOT); }
        SB();
#pragma unroll
        for (int s = 0; s < 28; ++s) {
            if (s + 2 < 28) { Bq[(s + 2) % 3][0] = lds128(ab + (s + 2) * 1024); Bq[(s + 2) % 3][1] = lds128(ab + (s + 2) * 1024 + SLOT); }
            acc0 = mfma32(A1[s], Bq[s % 3][0], acc0);
            acc1 = mfma32(A1[s], Bq[s % 3][1], acc1);
            SB();
        }
        __builtin_amdgcn_s_setprio(0);
        STAMP(8);
        if (w >= 4) { WAITV(0); BAR(); }
        const u32 vg = WS_G + ((cl * 4 + (tok32 >> 4)) * 56 + j * 8 + w) * 1024 + (hh * 16 + i16) * 16;
#pragma unroll
        for (int tp = 0; tp < 2; ++tp)
#pragma unroll
            for (int be = 0; be < 2; ++be) {
                v4u o;
#pragma unroll
                for (int e = 0; e < 4; ++e) {
                    const int r = 8 * be + 2 * e;
                    const float u0 = tp ? acc1[r] : acc0[r], u1 = tp ? acc1[r + 1] : acc0[r + 1];
                    o[e] = pk2(gelu_tanh(u0), gelu_tanh(u1));
                }
                st16c(rsW, vg + tp * (2 * 56 * 1024) + be * 512, o);
            }
        WAITV(0);
        put_flag(rsW, WS_FG + (cl * 8 + w) * 128 + j * 8, ep, lane);
        STAMP(9);
        if (w < 4) {
            BAR();
#pragma unroll
            for (int i = 0; i < 7; ++i) { int n = w + 4 * i + 7 * c; n = n >= 28 ? n - 28 : n; w2_piece(3, n, 0); }
#pragma unroll
            for (int i = 0; i < 7; ++i) { int n = w + 4 * i + 7 * c; n = n >= 28 ? n - 28 : n; w2_piece(4, n, 1); }
        }
    }

    {
        u32 a2[4];
        const int row = 16 * (g4 >> 1) + 4 * (g4 & 1) + q4;
#pragma unroll
        for (int ct = 0; ct < 4; ++ct) a2[ct] = row * 128 + (((ct ^ ((row >> 1) & 3)) << 1) | (p4 >> 1)) * 16 + (p4 & 1) * 8;
        f4 acc[4] = {};
        const u32 vg = WS_G + (cl * 4 + t4) * (56 * 1024) + lane * 16;
        const u32 vf = WS_FG + cl * 8 * 128 + (lane < 7 ? wh * 128 + lane * 8 : (wh + 2) * 128 + ((lane - 7) & 7) * 8);
        v4u ga[7], gb[7];
        auto load7 = [&](v4u (&gfr)[7], int phi) {
            const u32 so = (u32)phi * 1024;
            gfr[0] = ld16c<0>(rsW, vg, so); gfr[1] = ld16c<0>(rsW, vg, so + 8192); gfr[2] = ld16c<0>(rsW, vg, so + 16384); gfr[3] = ld16c<0>(rsW, vg, so + 24576);
            gfr[4] = ld16c<0>(rsW, vg, so + 32768); gfr[5] = ld16c<0>(rsW, vg, so + 40960); gfr[6] = ld16c<0>(rsW, vg, so + 49152);
        };
        auto quarter = [&](v4u (&gfr)[7], int slot) {
            const u32 sb = (u32)slot * SLOT;
#pragma unroll
            for (int p = 0; p < 7; ++p)
#pragma unroll
                for (int ct = 0; ct < 4; ++ct) {
                    const u32 a = a2[ct] + sb + p * 4096;
                    acc[ct] = mfma16(trfrag(a, a + 1024), asbf(gfr[p]), acc[ct]);
                }
        };
        poll_flags(rsW, vf, 14, ep, lane);
        load7(ga, wh); load7(gb, 2 + wh);
        STAMP(10);
        WAITV(7); PIN7(ga);
        quarter(ga, wh ? 3 : 2);
        BAR();
        {
#pragma unroll
            for (int i = 0; i < 4; ++i) { const int n0 = w + 8 * i; if (n0 < 28) { int n = n0 + 7 * c; n = n >= 28 ? n - 28 : n; w2_piece(5, n, 2); } }
#pragma unroll
            for (int i = 0; i < 4; ++i) { const int n0 = w + 8 * i; if (n0 < 28) { int n = n0 + 7 * c; n = n >= 28 ? n - 28 : n; w2_piece(6, n, 3); } }
        }
        if (w < 4) WAITV(8); else WAITV(6);
        PIN7(gb);
        quarter(gb, wh ? 0 : 4);
        poll_flags(rsW, vf + 4 * 128, 14, ep, lane);
        load7(ga, 4 + wh); load7(gb, 6 + wh);
        BAR();
        STAMP(11);
        w2_slot(7, 4);
        if (w < 4) WAITV(11); else WAITV(10);
        PIN7(ga);
        quarter(ga, wh ? 2 : 1);
        WAITV(0); PIN7(gb);
        BAR();
        quarter(gb, wh ? 4 : 3);
        STAMP(12);
        f4 keep0, keep1, send0, send1;
        if (wh == 0) {
            keep0 = acc[0] + (f4){blo(hp[0].x), bhi(hp[0].x), blo(hp[0].y), bhi(hp[0].y)};
            keep1 = acc[1] + (f4){blo(hp[1].x), bhi(hp[1].x), blo(hp[1].y), bhi(hp[1].y)};
            send0 = acc[2] + (f4){blo(hp[2].x), bhi(hp[2].x), blo(hp[2].y), bhi(hp[2].y)};
            send1 = acc[3] + (f4){blo(hp[3].x), bhi(hp[3].x), blo(hp[3].y), bhi(hp[3].y)};
        } else { keep0 = acc[2]; keep1 = acc[3]; send0 = acc[0]; send1 = acc[1]; }
        *(LAS f4*)(L_RED + w * 2048 + lane * 16) = send0;
        *(LAS f4*)(L_RED + w * 2048 + 1024 + lane * 16) = send1;
        BAR();
        keep0 += *(LAS f4*)(L_RED + (w ^ 4) * 2048 + lane * 16) + *(LAS f4*)(L_B2 + (32 * wh + 4 * g4) * 4);
        keep1 += *(LAS f4*)(L_RED + (w ^ 4) * 2048 + 1024 + lane * 16) + *(LAS f4*)(L_B2 + (32 * wh + 16 + 4 * g4) * 4);
        const u32 a0 = pk2(keep0[0], keep0[1]), a1 = pk2(keep0[2], keep0[3]), c0 = pk2(keep1[0], keep1[1]), c1 = pk2(keep1[2], keep1[3]);
        const auto s0 = __builtin_amdgcn_permlane16_swap(a0, c0, false, false);
        const auto s1 = __builtin_amdgcn_permlane16_swap(a1, c1, false, false);
        const v4u o = {s0[0], s1[0], s0[1], s1[1]};
        *(GAS v4u*)((char*)outp + (size_t)(r0 + 16 * t4 + i16) * 896 + j * 128 + wh * 64 + (g4 & 1) * 32 + (g4 >> 1) * 16) = o;
        STAMP(13);
    }
}

extern "C" void kernel_launch(void* const* d_in, const int* in_sizes, int n_in, void* d_out, int out_size, void* d_ws, size_t ws_size,
                              hipStream_t stream) {
    (void)in_sizes; (void)n_in; (void)out_size; (void)ws_size;
    (void)hipFuncSetAttribute((const void*)fused_block, hipFuncAttributeMaxDynamicSharedMemorySize, LDS_BYTES);
    hipLaunchKernelGGL(fused_block, dim3(256), dim3(512), LDS_BYTES, stream, (const u16*)d_in[0], (const u16*)d_in[1], (const u16*)d_in[2],
                       (const u16*)d_in[4], (const u16*)d_in[5], (const u16*)d_in[6], (const u16*)d_in[7], (u16*)d_out, (char*)d_ws);
}
```

```cpp
#include <hip/hip_runtime.h>
#include <hip/hip_bf16.h>
#include <stdint.h>

typedef unsigned short u16;
typedef unsigned int u32;
typedef unsigned long long u64;
typedef u32 v4u __attribute__((ext_vector_type(4)));
typedef u32 v2u __attribute__((ext_vector_type(2)));
typedef float f2 __attribute__((ext_vector_type(2)));
typedef float f4 __attribute__((ext_vector_type(4)));
typedef float f16v __attribute__((ext_vector_type(16)));
typedef __bf16 bf8 __attribute__((ext_vector_type(8)));
typedef __bf16 bf2 __attribute__((ext_vector_type(2)));
typedef short s4 __attribute__((ext_vector_type(4)));
typedef short s8 __attribute__((ext_vector_type(8)));
typedef __amdgpu_buffer_rsrc_t rsrc_t;
#define LAS __attribute__((address_space(3)))
#define GAS __attribute__((address_space(1)))
#define DI __device__ __forceinline__
template <int N> struct IC { static constexpr int value = N; };

#define WARM 1
#define PSLEEP 2
#define GMARK 1
#define PREFETCH 1
#define ROTA 1
#define CODEPF 1
constexpr int S_ = 2048, D_ = 448, F_ = 1792;
constexpr u32 FL = 128;
constexpr u32 WS_GEN = 0;
constexpr u32 WS_FP = 32 * FL;
constexpr u32 WS_FG = WS_FP + 64 * FL;
constexpr u32 WS_O = 65536;
constexpr u32 HS_BYTES = 61440;
constexpr u32 WS_H = WS_O + 32 * 65536;
constexpr u32 WS_G = WS_H + 32 * HS_BYTES;
constexpr u32 WS_STAMP = WS_G + 32 * 229376;
constexpr u32 WS_BYTES = WS_STAMP + 256 * 8 * 32 * 8;
constexpr u32 EMPTY = 0xffffffffu;
constexpr u32 OOB = 0x7fffff00u;
constexpr int SLOT = 28672;
constexpr int L_RING = 0, RING_SLOT = 22528;
constexpr int L_XN = 90112;
constexpr int L_QKV = 114688, QKV_RS = 400;
constexpr int L_WO = 0;
constexpr int L_STA = 57344;
constexpr int L_STB = 0;
constexpr int L_STC = 57344;
constexpr int CODE_PF_BYTES = 28672;
constexpr int L_HN = 0;
constexpr int L_RED = 143360;
constexpr int L_CTL = 161792;
constexpr int L_B1 = L_CTL, L_B2 = L_CTL + 1024, L_EP = L_CTL + 1280;
constexpr int L_CNT = L_CTL + 1296;
constexpr int L_DUMMY = L_CTL + 1536;
constexpr int LDS_BYTES = 163840;
constexpr u32 SPIN_CAP = 1u << 15;

DI u32 pk2(float lo, float hi) { f2 v = {lo, hi}; return __builtin_bit_cast(u32, __builtin_convertvector(v, bf2)); }
DI float blo(u32 x) { return __uint_as_float(x << 16); }
DI float bhi(u32 x) { return __uint_as_float(x & 0xffff0000u); }
template <int CTRL> DI float dpp(float x) { return __builtin_bit_cast(float, __builtin_amdgcn_mov_dpp(__builtin_bit_cast(int, x), CTRL, 0xf, 0xf, true)); }
DI float sum8(float x) { x += dpp<0xB1>(x); x += dpp<0x4E>(x); x += dpp<0x141>(x); return x; }
DI float xsum16(float x) { auto s = __builtin_amdgcn_permlane16_swap(__float_as_uint(x), __float_as_uint(x), false, false); return __uint_as_float(s[0]) + __uint_as_float(s[1]); }
DI float xsum32(float x) { auto s = __builtin_amdgcn_permlane32_swap(__float_as_uint(x), __float_as_uint(x), false, false); return __uint_as_float(s[0]) + __uint_as_float(s[1]); }
DI float xmax16(float x) { auto s = __builtin_amdgcn_permlane16_swap(__float_as_uint(x), __float_as_uint(x), false, false); return fmaxf(__uint_as_float(s[0]), __uint_as_float(s[1])); }
DI float xmax32(float x) { auto s = __builtin_amdgcn_permlane32_swap(__float_as_uint(x), __float_as_uint(x), false, false); return fmaxf(__uint_as_float(s[0]), __uint_as_float(s[1])); }

DI bf8 trfrag(u32 a0, u32 a1) {
    s4 x = __builtin_amdgcn_ds_read_tr16_b64_v4i16((LAS s4*)(size_t)a0);
    s4 y = __builtin_amdgcn_ds_read_tr16_b64_v4i16((LAS s4*)(size_t)a1);
    s8 r = __builtin_shufflevector(x, y, 0, 1, 2, 3, 4, 5, 6, 7);
    return __builtin_bit_cast(bf8, r);
}
DI bf8 lds128(u32 a) { return __builtin_bit_cast(bf8, *(LAS v4u*)(size_t)a); }
DI bf8 asbf(v4u x) { return __builtin_bit_cast(bf8, x); }
DI f4 mfma16(bf8 a, bf8 b, f4 c) { return __builtin_amdgcn_mfma_f32_16x16x32_bf16(a, b, c, 0, 0, 0); }

#define BAR() do { asm volatile("" ::: "memory"); __builtin_amdgcn_s_waitcnt(0xC07F); __builtin_amdgcn_s_barrier(); asm volatile("" ::: "memory"); } while (0)
#define WAITV(n) asm volatile("s_waitcnt vmcnt(" #n ")" ::: "memory")
#define SB() __builtin_amdgcn_sched_barrier(0)

DI void dma16(rsrc_t rs, u32 voff, u32 soff, u32 lds) {
    asm volatile("s_mov_b32 m0, %0\n\ts_nop 0\n\tbuffer_load_dwordx4 %1, %2, %3 offen lds" ::"s"(lds), "v"(voff), "s"(rs), "s"(soff) : "memory");
}
template <int N> DI void dma_run(rsrc_t rs, u32 voff, u32 soff, u32 sstep, u32 lds, u32 lstep) {
    u32 t;
    asm volatile("s_mov_b32 m0, %3\n\ts_mov_b32 %0, %2\n\t.rept %7\n\tbuffer_load_dwordx4 %1, %4, %0 offen lds\n\ts_add_u32 m0, m0, %6\n\ts_add_u32 %0, %0, %5\n\t.endr"
                 : "=&s"(t) : "v"(voff), "s"(soff), "s"(lds), "s"(rs), "s"(sstep), "s"(lstep), "i"(N) : "memory", "scc");
}
DI void touch4(rsrc_t rs, u32 voff, u32 lds) {
    asm volatile("s_mov_b32 m0, %0\n\ts_nop 0\n\tbuffer_load_dword %1, %2, 0 offen lds" ::"s"(lds), "v"(voff), "s"(rs) : "memory");
}
template <int OFF> DI v4u ld16(rsrc_t rs, u32 voff, u32 soff) {
    v4u r; asm volatile("buffer_load_dwordx4 %0, %1, %2, %3 offen offset:%4" : "=v"(r) : "v"(voff), "s"(rs), "s"(soff), "i"(OFF) : "memory"); return r;
}
template <int OFF> DI v4u ld16c(rsrc_t rs, u32 voff, u32 soff) {
    v4u r; asm volatile("buffer_load_dwordx4 %0, %1, %2, %3 offen offset:%4 sc1" : "=v"(r) : "v"(voff), "s"(rs), "s"(soff), "i"(OFF) : "memory"); return r;
}
template <int OFF> DI v2u ld8(rsrc_t rs, u32 voff, u32 soff) {
    v2u r; asm volatile("buffer_load_dwordx2 %0, %1, %2, %3 offen offset:%4" : "=v"(r) : "v"(voff), "s"(rs), "s"(soff), "i"(OFF) : "memory"); return r;
}
template <int OFF> DI v2u ld8c(rsrc_t rs, u32 voff, u32 soff) {
    v2u r; asm volatile("buffer_load_dwordx2 %0, %1, %2, %3 offen offset:%4 sc1" : "=v"(r) : "v"(voff), "s"(rs), "s"(soff), "i"(OFF) : "memory"); return r;
}
DI void st16c(rsrc_t rs, u32 voff, v4u v) { __builtin_amdgcn_raw_buffer_store_b128(v, rs, voff, 0, 16); }
DI void st8c(rsrc_t rs, u32 voff, v2u v) { __builtin_amdgcn_raw_buffer_store_b64(v, rs, voff, 0, 16); }
#define PIN1(a) asm volatile("" : "+v"(a))
#define PIN4(a) asm volatile("" : "+v"((a)[0]), "+v"((a)[1]), "+v"((a)[2]), "+v"((a)[3]))
#define PIN7(a) asm volatile("" : "+v"((a)[0]), "+v"((a)[1]), "+v"((a)[2]), "+v"((a)[3]), "+v"((a)[4]), "+v"((a)[5]), "+v"((a)[6]))

DI u32 nz(u32 x) { return x < EMPTY - 1 ? x : EMPTY - 1; }
DI v4u nz4(v4u v) { return (v4u){nz(v.x), nz(v.y), nz(v.z), nz(v.w)}; }
DI u32 mx4(u32 m, v4u v) { const u32 a = m > v.x ? m : v.x, b = a > v.y ? a : v.y, c = b > v.z ? b : v.z; return c > v.w ? c : v.w; }
DI void poll_written(rsrc_t rs, u32 voff) {
#pragma unroll 1
    for (u32 sp = 0; sp < SPIN_CAP; ++sp) {
        v2u f;
        asm volatile("buffer_load_dwordx2 %0, %1, %2, 0 offen sc1\n\ts_waitcnt vmcnt(0)" : "=&v"(f) : "v"(voff), "s"(rs) : "memory");
        if (!__any(f.x == EMPTY || f.y == EMPTY)) break;
        __builtin_amdgcn_s_sleep(PSLEEP);
    }
}
DI u32 mx2(u32 a, u32 b) { return a > b ? a : b; }
DI void poll_flags(rsrc_t rs, u32 voff, int nflags, u64 ep, int lane) {
    const bool act = lane < nflags;
    voff = act ? voff : OOB;
    const u32 elo = (u32)ep, ehi = (u32)(ep >> 32);
#pragma unroll 1
    for (u32 sp = 0; sp < SPIN_CAP; ++sp) {
        v2u f;
        asm volatile("buffer_load_dwordx2 %0, %1, %2, 0 offen sc1\n\ts_waitcnt vmcnt(0)" : "=&v"(f) : "v"(voff), "s"(rs) : "memory");
        const bool ok = !act || (f.x == elo && f.y == ehi);
        if (__all(ok)) break;
        __builtin_amdgcn_s_sleep(1);
    }
}
DI void put_flag(rsrc_t rs, u32 off, u64 ep, int lane) {
    if (lane == 0) st8c(rs, off, (v2u){(u32)ep, (u32)(ep >> 32)});
}

#define STAMP(k) asm volatile("; MARK " #k)
#define STAMPP(k) do { } while (0)
#define STAMPC(k) do { } while (0)
#define STAMPS_DECL do { } while (0)
#define STAMPS_OUT() do { } while (0)

DI void ln1_group(v4u (&xv)[7], int grp, int lane) {
    f2 v[28];
#pragma unroll
    for (int c = 0; c < 7; ++c)
#pragma unroll
        for (int e = 0; e < 4; ++e) v[4 * c + e] = (f2){blo(xv[c][e]), bhi(xv[c][e])};
    f2 s[4], q[4];
#pragma unroll
    for (int e = 0; e < 4; ++e) { s[e] = v[e]; q[e] = v[e] * v[e]; }
#pragma unroll
    for (int i = 4; i < 28; ++i) { s[i & 3] += v[i]; q[i & 3] += v[i] * v[i]; }
    const f2 ss = (s[0] + s[1]) + (s[2] + s[3]), qq = (q[0] + q[1]) + (q[2] + q[3]);
    const float mean = sum8(ss.x + ss.y) * (1.0f / 448.0f);
    const float var = fmaxf(sum8(qq.x + qq.y) * (1.0f / 448.0f) - mean * mean, 0.f);
    const float rstd = __builtin_amdgcn_rsqf(var + 1e-5f), nb = -mean * rstd;
    const f2 r2 = {rstd, rstd}, n2 = {nb, nb};
    const int gi = lane & 7, R = 8 * grp + (lane >> 3), gp = gi & 3;
    const u32 a = L_XN + ((R >> 4) * 14 + (gi >> 2)) * 1024 + (2 * (gp & 1) * 16 + ((R & 15) ^ (2 * (2 * (gi >> 2) + (gp & 1))))) * 16 + (gp >> 1) * 8;
#pragma unroll
    for (int c = 0; c < 7; ++c) {
        const f2 y0 = v[4 * c] * r2 + n2, y1 = v[4 * c + 1] * r2 + n2, y2 = v[4 * c + 2] * r2 + n2, y3 = v[4 * c + 3] * r2 + n2;
        *(LAS v2u*)(size_t)(a + c * 2048) = (v2u){pk2(y0.x, y0.y), pk2(y1.x, y1.y)};
        *(LAS v2u*)(size_t)(a + c * 2048 + 256) = (v2u){pk2(y2.x, y2.y), pk2(y3.x, y3.y)};
    }
}

DI v2u gelu4_pk(float u0, float u1, float u2, float u3, float k1) {
    float a, b, c, d; v2u r;
    asm("v_mul_f32 %0, %6, %6\n\tv_mul_f32 %1, %7, %7\n\tv_mul_f32 %2, %8, %8\n\tv_mul_f32 %3, %9, %9\n\t"
        "v_fmaak_f32 %0, %0, %10, 0xc0135761\n\tv_fmaak_f32 %1, %1, %10, 0xc0135761\n\tv_fmaak_f32 %2, %2, %10, 0xc0135761\n\tv_fmaak_f32 %3, %3, %10, 0xc0135761\n\t"
        "v_mul_f32 %0, %6, %0\n\tv_mul_f32 %1, %7, %1\n\tv_mul_f32 %2, %8, %2\n\tv_mul_f32 %3, %9, %3\n\t"
        "v_exp_f32 %0, %0\n\tv_exp_f32 %1, %1\n\tv_exp_f32 %2, %2\n\tv_exp_f32 %3, %3\n\t"
        "v_add_f32 %0, 1.0, %0\n\tv_add_f32 %1, 1.0, %1\n\tv_add_f32 %2, 1.0, %2\n\tv_add_f32 %3, 1.0, %3\n\t"
        "v_rcp_f32 %0, %0\n\tv_rcp_f32 %1, %1\n\tv_rcp_f32 %2, %2\n\tv_rcp_f32 %3, %3\n\t"
        "v_mul_f32 %0, %6, %0\n\tv_mul_f32 %1, %7, %1\n\tv_mul_f32 %2, %8, %2\n\tv_mul_f32 %3, %9, %3\n\t"
        "v_cvt_pk_bf16_f32 %4, %0, %1\n\tv_cvt_pk_bf16_f32 %5, %2, %3"
        : "=&v"(a), "=&v"(b), "=&v"(c), "=&v"(d), "=&v"(r.x), "=&v"(r.y)
        : "v"(u0), "v"(u1), "v"(u2), "v"(u3), "v"(k1));
    return r;
}

__global__ void __launch_bounds__(512, 2)
fused_block(const u16* __restrict__ xp, const u16* __restrict__ wqkvp, const u16* __restrict__ wop, const u16* __restrict__ w1p,
            const u16* __restrict__ b1p, const u16* __restrict__ w2p, const u16* __restrict__ b2p, u16* __restrict__ outp, char* __restrict__ wsp) {
    u64 pc; asm volatile("s_getpc_b64 %0" : "=s"(pc));
    const int tid = threadIdx.x, lane = tid & 63;
    const int w = __builtin_amdgcn_readfirstlane(tid >> 6);
    const int b = blockIdx.x, xi = b >> 3, c = xi >> 3, j = xi & 7, cl = (b & 7) * 4 + c, r0 = cl * 64;
    if (xi < 4 && w == 7) {
        const rsrc_t rsC = __builtin_amdgcn_make_buffer_rsrc((void*)(pc & ~127ull), 0, CODE_PF_BYTES, 0x00020000);
        touch4(rsC, (u32)(xi * 64 + lane) * 128, L_DUMMY);
    }
    const rsrc_t rsX = __builtin_amdgcn_make_buffer_rsrc((void*)xp, 0, S_ * D_ * 2, 0x00020000);
    const rsrc_t rsW = __builtin_amdgcn_make_buffer_rsrc((void*)wsp, 0, WS_BYTES, 0x00020000);
    STAMPS_DECL;
    STAMP(0); STAMPC(30);
    const int g4 = lane >> 4, q4 = (lane & 15) >> 2, p4 = lane & 3, i16 = lane & 15;
    const rsrc_t rsO = __builtin_amdgcn_make_buffer_rsrc((void*)wop, 0, D_ * D_ * 2, 0x00020000);
    const rsrc_t rs1 = __builtin_amdgcn_make_buffer_rsrc((void*)w1p, 0, D_ * F_ * 2, 0x00020000);
    const u32 swz = (u32)(((lane & 7) ^ (((lane >> 4) & 3) << 1)) * 16);
    const u32 vw1 = (u32)((lane >> 3) * 3584 + j * 512) + swz;
    auto w1_run = [&](auto nkb, int kb0, int lbase) {
        constexpr int NKB = decltype(nkb)::value;
#pragma unroll
        for (int sg = 0; sg < 4; ++sg) {
            const int q = ROTA ? ((sg + c) & 3) : sg;
            dma_run<NKB>(rs1, vw1, (u32)((kb0 * 32 + q * NKB * 8) * 3584 + (w - 4) * 128), 8 * 3584, (u32)(lbase + ((w - 4) * 4 + q) * NKB * 1024), 1024);
        }
    };
    const v4u empty4 = {EMPTY, EMPTY, EMPTY, EMPTY};
    const int k0 = (2 * c + (j & 1)) % 7;

    if (w < 6) {
        v4u xa[7], xb[7];
        const int ra = r0 - 16 + 8 * w + (lane >> 3);
        const u32 va = (u32)(ra < 0 ? 0 : ra) * 896 + (lane & 7) * 16;
        xa[0] = ld16<0>(rsX, va, 0); xa[1] = ld16<128>(rsX, va, 0); xa[2] = ld16<256>(rsX, va, 0); xa[3] = ld16<384>(rsX, va, 0);
        xa[4] = ld16<512>(rsX, va, 0); xa[5] = ld16<640>(rsX, va, 0); xa[6] = ld16<768>(rsX, va, 0);
        if (w < 4) {
            const u32 vb = (u32)(r0 + 32 + 8 * w + (lane >> 3)) * 896 + (lane & 7) * 16;
            xb[0] = ld16<0>(rsX, vb, 0); xb[1] = ld16<128>(rsX, vb, 0); xb[2] = ld16<256>(rsX, vb, 0); xb[3] = ld16<384>(rsX, vb, 0);
            xb[4] = ld16<512>(rsX, vb, 0); xb[5] = ld16<640>(rsX, vb, 0); xb[6] = ld16<768>(rsX, vb, 0);
            STAMP(14);
            WAITV(7);
        } else {
            STAMP(14);
            WAITV(0);
        }
        STAMP(15);
        PIN7(xa);
        ln1_group(xa, w, lane);
        if (w < 4) { WAITV(0); PIN7(xb); ln1_group(xb, 6 + w, lane); }
        STAMP(1);
        auto p1_loop = [&](auto ncc, auto ntc, const int c0, const int T0) {
            constexpr int NC = decltype(ncc)::value, NT = decltype(ntc)::value;
            const u32 abase = L_RING + (4 * g4 + q4) * 352 + p4 * 8 + 32 * c0;
            u32 bb[2];
#pragma unroll
            for (int s = 0; s < 2; ++s) bb[s] = L_XN + T0 * 14336 + s * 1024 + (g4 * 16 + (i16 ^ (2 * (2 * s + (g4 >> 1))))) * 16;
            f4 acc[NC][NT] = {};
            bf8 Ap[NC] = {}, Bp[NT] = {};
            int kc = k0;
#pragma unroll 1
            for (int i = 0; i < 7; ++i) {
                BAR();
                STAMPP(17 + i);
                const u32 aa = abase + ((i + 1) & 3) * RING_SLOT, bo = kc * 2048;
                kc = kc == 6 ? 0 : kc + 1;
                bf8 A[NC], B[NT];
#pragma unroll
                for (int n = 0; n < NC * NT; ++n) {
                    acc[n / NT][n % NT] = mfma16(Ap[n / NT], Bp[n % NT], acc[n / NT][n % NT]);
                    if (n == 0) A[0] = trfrag(aa, aa + 5632);
                    else if (n <= NT) B[n - 1] = lds128(bb[0] + bo + (n - 1) * 14336);
                    else if (n < NC + NT) A[n - NT] = trfrag(aa + 32 * (n - NT), aa + 32 * (n - NT) + 5632);
                    SB();
                }
#pragma unroll
                for (int n = 0; n < NC * NT; ++n) {
                    acc[n / NT][n % NT] = mfma16(A[n / NT], B[n % NT], acc[n / NT][n % NT]);
                    if (n == 0) Ap[0] = trfrag(aa + 11264, aa + 11264 + 5632);
                    else if (n <= NT) Bp[n - 1] = lds128(bb[1] + bo + (n - 1) * 14336);
                    else if (n < NC + NT) Ap[n - NT] = trfrag(aa + 11264 + 32 * (n - NT), aa + 11264 + 32 * (n - NT) + 5632);
                    SB();
                }
            }
#pragma unroll
            for (int n = 0; n < NC * NT; ++n) acc[n / NT][n % NT] = mfma16(Ap[n / NT], Bp[n % NT], acc[n / NT][n % NT]);
            BAR();
            STAMP(2);
#pragma unroll
            for (int cc = 0; cc < NC; ++cc) {
                const int col = 16 * (c0 + cc) + 4 * g4, part = col >= 112 ? 2 : (col >= 56 ? 1 : 0), rem = col - 56 * part, head = rem >= 28 ? 1 : 0;
                const u32 a = L_QKV + (16 * T0 + i16) * QKV_RS + (2 * part + head) * 64 + (rem - 28 * head) * 2;
                if (col < 168) {
#pragma unroll
                    for (int t = 0; t < NT; ++t)
                        *(LAS v2u*)(size_t)(a + t * 16 * QKV_RS) = (v2u){pk2(acc[cc][t][0], acc[cc][t][1]), pk2(acc[cc][t][2], acc[cc][t][3])};
                }
            }
        };
        if (w == 2 || w == 3) p1_loop(IC<3>{}, IC<5>{}, 3 * (w - 1), 0);
        else if (w == 0 || w == 4) p1_loop(IC<3>{}, IC<2>{}, 0, 1 + (w >> 1));
        else if (w == 1) p1_loop(IC<2>{}, IC<3>{}, 9, 0);
        else p1_loop(IC<2>{}, IC<2>{}, 9, 3);
    } else {
        const rsrc_t rsQ = __builtin_amdgcn_make_buffer_rsrc((void*)wqkvp, 0, D_ * 3 * D_ * 2, 0x00020000);
        const int lw = w - 6;
        u32 vq[11];
#pragma unroll
        for (int i = 0; i < 11; ++i) {
            const int m = 64 * (lw + 2 * i) + lane, row = m / 22, gr = m - row * 22;
            const int part = gr < 7 ? 0 : (gr < 14 ? 1 : 2); int sub = gr - 7 * part; sub = sub > 6 ? 6 : sub;
            vq[i] = (u32)(row * 2688 + part * 896 + j * 112 + sub * 16);
        }
        auto issue_chunk = [&](int i) {
            int kc = k0 + i; kc = kc >= 7 ? kc - 7 : kc;
            const u32 so = (u32)kc * (64 * 2688), ld = L_RING + ((i + 1) & 3) * RING_SLOT + lw * 1024;
#pragma unroll
            for (int p = 0; p < 11; ++p) dma16(rsQ, vq[p], so, ld + p * 2048);
        };
        v2u gen = {0u, 0u};
        if (lw == 1) gen = ld8c<0>(rsW, WS_GEN + cl * FL, 0);
        issue_chunk(0); issue_chunk(1); issue_chunk(2);
        STAMP(14);
        WAITV(22);
        if (lw == 1) { PIN1(gen); if (lane == 0) { *(LAS u64*)(size_t)(L_EP) = (((u64)gen.y << 32) | gen.x) + 1; *(LAS u32*)(size_t)(L_CNT) = 0u; } }
        STAMP(1);
        const bool marker = j == 7 || j == 0;
        const u32 mk0 = (j == 7 ? WS_O + cl * 65536 + lw * 32768 : WS_H + cl * HS_BYTES + lw * 30720) + lane * 16;
        const int mkn = j == 7 ? 32 : 30;
        const u32 vo = (u32)((lane >> 3) * 896 + j * 128) + swz;
        auto wo_run = [&](auto cnt, int n0) {
            dma_run<decltype(cnt)::value>(rsO, j < 7 ? vo : OOB, (u32)n0 * 7168, 2 * 7168, (u32)(L_WO + n0 * 1024), 2048);
        };
#pragma unroll 1
        for (int i = 0; i < 4; ++i) {
            WAITV(22);
            STAMPP(17 + i);
            BAR();
            issue_chunk(i + 3);
            if (marker && i < 2) {
#pragma unroll 1
                for (int p = 16 * i; p < (i ? mkn : 16); ++p) st16c(rsW, mk0 + p * 1024, empty4);
            }
        }
        WAITV(22); STAMPP(21); BAR();
        const u64 ep0 = *(LAS u64*)(size_t)(L_EP);
        if (marker && lw == 0 && lane == 0) st8c(rsW, WS_FP + (cl * 2 + (j == 0)) * FL, (v2u){(u32)ep0, (u32)(ep0 >> 32)});
        v2u b1v = {0u, 0u}, b2v = {0u, 0u};
        {
            const rsrc_t rsb1 = __builtin_amdgcn_make_buffer_rsrc((void*)b1p, 0, F_ * 2, 0x00020000);
            const rsrc_t rsb2 = __builtin_amdgcn_make_buffer_rsrc((void*)b2p, 0, D_ * 2, 0x00020000);
            b1v = ld8<0>(rsb1, lw == 0 ? j * 512 + lane * 8 : OOB, 0);
            b2v = ld8<0>(rsb2, lw == 0 ? j * 128 + (lane & 15) * 8 : OOB, 0);
        }
        wo_run(IC<11>{}, lw);
        WAITV(22); STAMPP(22); BAR();
        wo_run(IC<11>{}, 22 + lw);
        v2u fchk = ld8c<0>(rsW, lane < 2 ? WS_FP + (cl * 2 + lane) * FL : OOB, 0);
        WAITV(22); STAMPP(23); BAR();
        wo_run(IC<6>{}, 44 + lw);
        PIN1(b1v); PIN1(b2v);
        if (lw == 0) {
            *(LAS f4*)(size_t)(L_B1 + lane * 16) = (f4){blo(b1v.x), bhi(b1v.x), blo(b1v.y), bhi(b1v.y)};
            if (lane < 16) *(LAS f4*)(size_t)(L_B2 + lane * 16) = (f4){blo(b2v.x), bhi(b2v.x), blo(b2v.y), bhi(b2v.y)};
        }
        WAITV(6); PIN1(fchk);
        if (!__all(lane >= 2 || (fchk.x == (u32)ep0 && fchk.y == (u32)(ep0 >> 32)))) poll_flags(rsW, WS_FP + (cl * 2 + lane) * FL, 2, ep0, lane);
        BAR();
        STAMP(2);
        {
            const rsrc_t rsT = __builtin_amdgcn_make_buffer_rsrc((void*)(lw == 0 ? w1p : w2p), 0, F_ * D_ * 2, 0x00020000);
            touch4(rsT, lane < 49 ? (u32)(b * 49 + lane) * 128 : OOB, L_DUMMY);
        }
#pragma unroll
        for (int n = 0; n < 4; ++n) {
            const int idx = lane + 64 * n, row = idx / 6;
            if (idx < 240) *(LAS v2u*)(size_t)(L_QKV + (40 * lw + row) * QKV_RS + (idx - 6 * row) * 64 + 56) = (v2u){0u, 0u};
        }
    }
    BAR();
    STAMP(16);
    const u64 ep = *(LAS u64*)(size_t)(L_EP);
    const int t4 = w & 3, wh = w >> 2;

    {
        const u32 rowq = L_QKV + (16 + 16 * t4 + i16) * QKV_RS + g4 * 16;
        const bf8 Qf = lds128(rowq + wh * 64);
        const bf8 K0 = lds128(rowq - 16 * QKV_RS + (2 + wh) * 64), K1 = lds128(rowq + (2 + wh) * 64);
        const u32 va = L_QKV + (16 * t4 + 4 * g4 + q4) * QKV_RS + (4 + wh) * 64 + p4 * 8;
        const bf8 V0 = trfrag(va, va + 16 * QKV_RS), V1 = trfrag(va + 32, va + 32 + 16 * QKV_RS);
        const f4 z = {0.f, 0.f, 0.f, 0.f};
        const f4 S0 = mfma16(K0, Qf, z), S1 = mfma16(K1, Qf, z);
        const float cs = 0.18898223650461362f * 1.4426950408889634f;
        float sv[8]; float mx = -3.0e38f;
        const int kbase = r0 - 16 + 16 * t4 + 4 * g4;
#pragma unroll
        for (int e = 0; e < 8; ++e) {
            const int kt = e >> 2, r = e & 3, delta = i16 + 16 - 16 * kt - 4 * g4 - r;
            const bool ok = delta >= 0 && delta <= 14 && !(delta & 1) && (kbase + 16 * kt + r) >= 0;
            sv[e] = ok ? (kt ? S1[r] : S0[r]) * cs : -3.0e38f;
            mx = fmaxf(mx, sv[e]);
        }
        mx = xmax32(xmax16(mx));
        float sum = 0.f;
#pragma unroll
        for (int e = 0; e < 8; ++e) { sv[e] = __builtin_amdgcn_exp2f(sv[e] - mx); sum += sv[e]; }
        const float inv = __builtin_amdgcn_rcpf(xsum32(xsum16(sum)));
        const v4u P = {pk2(sv[0] * inv, sv[1] * inv), pk2(sv[2] * inv, sv[3] * inv), pk2(sv[4] * inv, sv[5] * inv), pk2(sv[6] * inv, sv[7] * inv)};
        const f4 O0 = mfma16(V0, asbf(P), z), O1 = mfma16(V1, asbf(P), z);
        const v4u og = {pk2(O0[0], O0[1]), pk2(O0[2], O0[3]), pk2(O1[0], O1[1]), pk2(O1[2], O1[3])};
        st16c(rsW, WS_O + ((cl * 4 + t4) * 16 + 2 * j + wh) * 1024 + lane * 16, nz4(og));
    }
    STAMP(3);
    if (j == 7) {
        {
#pragma unroll 1
            for (int n = w; n < 28; n += 8) {
                const int idx = (n * 4 + c) * 64 + lane, row = idx / 28;
                touch4(rs1, (u32)((192 + row) * 3584 + (idx - row * 28) * 128), L_DUMMY);
            }
            const rsrc_t rs2p = __builtin_amdgcn_make_buffer_rsrc((void*)w2p, 0, F_ * D_ * 2, 0x00020000);
#pragma unroll 1
            for (int n = w; n < 49; n += 8) {
                const int idx = (n * 4 + c) * 64 + lane, blk = idx / 224, rem = idx - blk * 224, r = rem / 7;
                const int phi = blk / 7, p = blk - 7 * phi;
                touch4(rs2p, (u32)((256 * p + 32 * phi + r) * 896 + (rem - 7 * r) * 128), L_DUMMY);
            }
        }
        STAMPS_OUT();
        return;
    }
    if (w >= 6) WAITV(1);
    BAR();

    bf8 A1[14][2];
    u32 a1lane[2];
    {
        const int row = 4 * g4 + q4;
#pragma unroll
        for (int ft = 0; ft < 2; ++ft) a1lane[ft] = (u32)(row * 128 + ((((2 * (w & 1) + ft) ^ ((row >> 1) & 3)) << 1) | (p4 >> 1)) * 16 + (p4 & 1) * 8);
    }
    auto w1_read = [&](auto nkb, int kb0, int lbase) {
        constexpr int NKB = decltype(nkb)::value;
#pragma unroll
        for (int ft = 0; ft < 2; ++ft) {
            const u32 a = lbase + (w >> 1) * NKB * 4096 + a1lane[ft];
#pragma unroll
            for (int r = 0; r < NKB; ++r) A1[kb0 + r][ft] = trfrag(a + r * 4096, a + r * 4096 + 2048);
        }
    };
    v2u hp[4];
    if (w < 4) {
        v4u xq[2];
        const u32 vx = (u32)(r0 + 16 * t4 + i16) * 896 + j * 128 + (g4 & 1) * 32 + (g4 >> 1) * 16;
        xq[0] = ld16<0>(rsX, vx, 0); xq[1] = ld16<64>(rsX, vx, 0);
        v4u of[16];
        const u32 vo = WS_O + (cl * 4 + t4) * 16384 + lane * 16;
        poll_written(rsW, WS_O + ((cl * 4 + t4) * 16 + i16) * 1024 + 1016);
        STAMP(18);
#pragma unroll 1
        for (u32 sp = 0; sp < SPIN_CAP; ++sp) {
            of[0] = ld16c<0>(rsW, vo, 0); of[1] = ld16c<1024>(rsW, vo, 0); of[2] = ld16c<2048>(rsW, vo, 0); of[3] = ld16c<3072>(rsW, vo, 0);
            of[4] = ld16c<0>(rsW, vo, 4096); of[5] = ld16c<1024>(rsW, vo, 4096); of[6] = ld16c<2048>(rsW, vo, 4096); of[7] = ld16c<3072>(rsW, vo, 4096);
            of[8] = ld16c<0>(rsW, vo, 8192); of[9] = ld16c<1024>(rsW, vo, 8192); of[10] = ld16c<2048>(rsW, vo, 8192); of[11] = ld16c<3072>(rsW, vo, 8192);
            of[12] = ld16c<0>(rsW, vo, 12288); of[13] = ld16c<1024>(rsW, vo, 12288); of[14] = ld16c<2048>(rsW, vo, 12288); of[15] = ld16c<3072>(rsW, vo, 12288);
            WAITV(0);
            PIN4(&of[0]); PIN4(&of[4]); PIN4(&of[8]); PIN4(&of[12]);
            u32 m[4] = {0u, 0u, 0u, 0u};
#pragma unroll
            for (int H = 0; H < 16; ++H) m[H & 3] = mx4(m[H & 3], of[H]);
            if (!__any(mx2(mx2(m[0], m[1]), mx2(m[2], m[3])) == EMPTY)) break;
        }
        PIN1(xq[0]); PIN1(xq[1]);
        v2u xr[4];
#pragma unroll
        for (int cp = 0; cp < 2; ++cp) {
            const auto s0 = __builtin_amdgcn_permlane16_swap(xq[cp].x, xq[cp].z, false, false);
            const auto s1 = __builtin_amdgcn_permlane16_swap(xq[cp].y, xq[cp].w, false, false);
            xr[2 * cp] = (v2u){s0[0], s1[0]}; xr[2 * cp + 1] = (v2u){s0[1], s1[1]};
        }
        STAMP(19);
        if (j == 0 && w == 0 && lane == 0) st8c(rsW, WS_GEN + cl * FL, (v2u){(u32)ep, (u32)(ep >> 32)});
        u32 ao[2][4];
        const int rr0 = 4 * g4 + q4, rr1 = (g4 < 3 ? 16 + 4 * g4 : 24) + q4;
#pragma unroll
        for (int ct = 0; ct < 4; ++ct) {
            ao[0][ct] = L_WO + rr0 * 128 + (((ct ^ ((rr0 >> 1) & 3)) << 1) | (p4 >> 1)) * 16 + (p4 & 1) * 8;
            ao[1][ct] = L_WO + rr1 * 128 + (((ct ^ ((rr1 >> 1) & 3)) << 1) | (p4 >> 1)) * 16 + (p4 & 1) * 8;
        }
        f4 acc[4] = {};
        bf8 Aw[3][4];
        auto rdA = [&](int H, int ct) { const int cx = ct ^ ((H & 1) << 1); Aw[H % 3][ct] = trfrag(ao[0][cx] + H * 3584, ao[1][cx] + H * 3584); };
#pragma unroll
        for (int ct = 0; ct < 4; ++ct) { rdA(0, ct); rdA(1, ct); }
        SB();
#pragma unroll
        for (int H = 0; H < 16; ++H)
#pragma unroll
            for (int ct = 0; ct < 4; ++ct) {
                if (H + 2 < 16) rdA(H + 2, ct);
                acc[ct] = mfma16(Aw[H % 3][ct], asbf(of[H]), acc[ct]);
                SB();
            }
        BAR();
        STAMP(5);
        float hv[16];
#pragma unroll
        for (int ct = 0; ct < 4; ++ct) {
            hp[ct] = (v2u){nz(pk2(acc[ct][0] + blo(xr[ct].x), acc[ct][1] + bhi(xr[ct].x))), nz(pk2(acc[ct][2] + blo(xr[ct].y), acc[ct][3] + bhi(xr[ct].y)))};
            hv[4 * ct] = blo(hp[ct].x); hv[4 * ct + 1] = bhi(hp[ct].x); hv[4 * ct + 2] = blo(hp[ct].y); hv[4 * ct + 3] = bhi(hp[ct].y);
        }
        const u32 hb = WS_H + cl * HS_BYTES;
        const u32 vh = hb + (t4 * 14 + 2 * j) * 1024 + lane * 16;
        st16c(rsW, vh, (v4u){hp[0].x, hp[0].y, hp[1].x, hp[1].y});
        st16c(rsW, vh + 1024, (v4u){hp[2].x, hp[2].y, hp[3].x, hp[3].y});
        float s = 0.f;
#pragma unroll
        for (int e = 0; e < 16; e += 4) s += (hv[e] + hv[e + 1]) + (hv[e + 2] + hv[e + 3]);
        const float m64 = xsum32(xsum16(s)) * (1.0f / 64.0f);
        float q = 0.f, q1 = 0.f;
#pragma unroll
        for (int e = 0; e < 16; e += 2) { const float d = hv[e] - m64, d1 = hv[e + 1] - m64; q += d * d; q1 += d1 * d1; }
        q = xsum32(xsum16(q + q1));
        if (g4 == 0) st8c(rsW, hb + 57344 + j * 512 + (16 * t4 + i16) * 8, (v2u){nz(__float_as_uint(m64)), nz(__float_as_uint(q))});
        STAMP(22);
        w1_read(IC<6>{}, 0, L_STA);
        BAR();
        STAMP(23);
        v4u hf[14]; v2u st[2];
        const u32 vs = hb + 57344 + (16 * t4 + i16) * 8 + 2 * g4 * 512;
        const u32 vs1 = g4 < 3 ? vs + 512 : vs;
        const u32 vhl = hb + t4 * 14336 + lane * 16;
        poll_written(rsW, (lane & 32) ? vs1 : vs);
        STAMP(6);
#pragma unroll 1
        for (u32 sp = 0; sp < SPIN_CAP; ++sp) {
            st[0] = ld8c<0>(rsW, vs, 0); st[1] = ld8c<0>(rsW, vs1, 0);
            hf[0] = ld16c<0>(rsW, vhl, 0); hf[1] = ld16c<1024>(rsW, vhl, 0); hf[2] = ld16c<2048>(rsW, vhl, 0); hf[3] = ld16c<3072>(rsW, vhl, 0);
            hf[4] = ld16c<0>(rsW, vhl, 4096); hf[5] = ld16c<1024>(rsW, vhl, 4096); hf[6] = ld16c<2048>(rsW, vhl, 4096); hf[7] = ld16c<3072>(rsW, vhl, 4096);
            hf[8] = ld16c<0>(rsW, vhl, 8192); hf[9] = ld16c<1024>(rsW, vhl, 8192); hf[10] = ld16c<2048>(rsW, vhl, 8192); hf[11] = ld16c<3072>(rsW, vhl, 8192);
            hf[12] = ld16c<0>(rsW, vhl, 12288); hf[13] = ld16c<1024>(rsW, vhl, 12288);
            WAITV(0);
            PIN7(&hf[0]); PIN7(&hf[7]); PIN1(st[0]); PIN1(st[1]);
            u32 m[4] = {mx4(0u, (v4u){st[0].x, st[0].y, st[1].x, st[1].y}), 0u, 0u, 0u};
#pragma unroll
            for (int f = 0; f < 14; ++f) m[f & 3] = mx4(m[f & 3], hf[f]);
            if (!__any(mx2(mx2(m[0], m[1]), mx2(m[2], m[3])) == EMPTY)) break;
        }
        STAMP(24);
        BAR();
        w1_read(IC<3>{}, 6, L_STB);
        w1_read(IC<5>{}, 9, L_STC);
        const bool v1 = g4 < 3;
        const float m0 = __uint_as_float(st[0].x), m1 = __uint_as_float(st[1].x);
        const float mean = xsum32(xsum16(m0 + (v1 ? m1 : 0.f))) * (1.0f / 7.0f);
        float t = __uint_as_float(st[0].y) + 64.0f * (m0 - mean) * (m0 - mean);
        if (v1) t += __uint_as_float(st[1].y) + 64.0f * (m1 - mean) * (m1 - mean);
        const float rstd = __builtin_amdgcn_rsqf(xsum32(xsum16(t)) * (1.0f / 448.0f) + 1e-5f);
        const float nb = -mean * rstd;
        const f2 r2 = {rstd, rstd}, n2 = {nb, nb};
#pragma unroll
        for (int f = 0; f < 14; ++f)
#pragma unroll
            for (int e = 0; e < 4; ++e) { const f2 y = (f2){blo(hf[f][e]), bhi(hf[f][e])} * r2 + n2; hf[f][e] = pk2(y.x, y.y); }
        STAMP(26);
        BAR();
        const u32 ah = L_HN + t4 * 14336 + lane * 16;
#pragma unroll
        for (int f = 0; f < 14; ++f) *(LAS v4u*)(size_t)(ah + f * 1024) = hf[f];
    } else {
        hp[0] = hp[1] = hp[2] = hp[3] = (v2u){0u, 0u};
        w1_run(IC<6>{}, 0, L_STA);
        STAMP(20);
        {
            const u32 mg = WS_G + ((cl * 4 + (w - 4)) * 56 + j * 8) * 1024 + lane * 16;
#pragma unroll
            for (int p = 0; p < 8 * GMARK; ++p) st16c(rsW, mg + p * 1024, empty4);
        }
        STAMP(4);
        WAITV(0);
        STAMP(21);
        BAR();
        STAMP(5);
        if (w == 4 && lane == 0) st8c(rsW, WS_FG + (cl * 8 + j) * FL, (v2u){(u32)ep, (u32)(ep >> 32)});
        w1_run(IC<3>{}, 6, L_STB);
        w1_read(IC<6>{}, 0, L_STA);
        BAR();
        w1_run(IC<5>{}, 9, L_STC);
        STAMP(6);
        WAITV(0);
        STAMP(25);
        BAR();
        w1_read(IC<3>{}, 6, L_STB);
        w1_read(IC<5>{}, 9, L_STC);
        BAR();
    }
    const rsrc_t rs2 = __builtin_amdgcn_make_buffer_rsrc((void*)w2p, 0, F_ * D_ * 2, 0x00020000);
    const u32 vw2 = (u32)((lane >> 3) * 896 + j * 128) + swz;
    auto w2_run = [&](int phi, int slot) {
        dma_run<7>(rs2, vw2, (u32)((32 * phi + 8 * t4) * 896), 256 * 896, (u32)(slot * SLOT + t4 * 1024), 4096);
    };
    const u32 vfg = WS_FG + (cl * 8 + (lane & 7)) * FL;
    BAR();
    STAMP(7);

    {
        if (w >= 4) {
            w2_run(0, 2); w2_run(1, 3); w2_run(2, 4);
            poll_flags(rsW, vfg, 7, ep, lane);
        }
        f4 acc[2][4];
#pragma unroll
        for (int ft = 0; ft < 2; ++ft) {
            const f4 bv = *(LAS f4*)(size_t)(L_B1 + (32 * w + 16 * ft + 4 * g4) * 4);
#pragma unroll
            for (int t = 0; t < 4; ++t) acc[ft][t] = bv;
        }
        if (w < 4) __builtin_amdgcn_s_setprio(1);
        const u32 ab = L_HN + lane * 16;
        bf8 Bq[3][4];
#pragma unroll
        for (int s = 0; s < 2; ++s)
#pragma unroll
            for (int t = 0; t < 4; ++t) Bq[s][t] = lds128(ab + (t * 14 + s) * 1024);
        SB();
#pragma unroll
        for (int s = 0; s < 14; ++s)
#pragma unroll
            for (int t = 0; t < 4; ++t) {
                if (s + 2 < 14) Bq[(s + 2) % 3][t] = lds128(ab + (t * 14 + s + 2) * 1024);
                acc[0][t] = mfma16(A1[s][0], Bq[s % 3][t], acc[0][t]);
                acc[1][t] = mfma16(A1[s][1], Bq[s % 3][t], acc[1][t]);
                SB();
            }
        __builtin_amdgcn_s_setprio(0);
        STAMP(8);
        __builtin_amdgcn_s_waitcnt(0xC07F);
        if (lane == 0) __hip_atomic_fetch_add((LAS u32*)(size_t)L_CNT, 1u, __ATOMIC_RELAXED, __HIP_MEMORY_SCOPE_WORKGROUP);
        const u32 vg = WS_G + (cl * 4 * 56 + j * 8 + w) * 1024 + lane * 16;
        const float gk1 = -2.0f * 0.7978845608028654f * 1.4426950408889634f * 0.044715f;
        asm volatile("s_nop 7\n\ts_nop 7" : "+v"(acc[0][0]), "+v"(acc[0][1]), "+v"(acc[0][2]), "+v"(acc[0][3]), "+v"(acc[1][0]), "+v"(acc[1][1]), "+v"(acc[1][2]), "+v"(acc[1][3]));
#pragma unroll
        for (int t = 0; t < 4; ++t) {
            const v2u lo = gelu4_pk(acc[0][t][0], acc[0][t][1], acc[0][t][2], acc[0][t][3], gk1);
            const v2u hi = gelu4_pk(acc[1][t][0], acc[1][t][1], acc[1][t][2], acc[1][t][3], gk1);
            st16c(rsW, vg + t * (56 * 1024), nz4((v4u){lo.x, lo.y, hi.x, hi.y}));
        }
        STAMP(27);
        if (w < 4) {
            poll_flags(rsW, vfg, 7, ep, lane);
#pragma unroll 1
            for (u32 sp = 0; sp < SPIN_CAP * 16; ++sp) {
                asm volatile("" ::: "memory");
                if (__builtin_amdgcn_readfirstlane(*(volatile LAS u32*)(size_t)L_CNT) >= 8) break;
                __builtin_amdgcn_s_sleep(1);
            }
            asm volatile("" ::: "memory");
            w2_run(3, 0); w2_run(4, 1);
        }
    }

    {
        u32 a2[4];
        const int row = 4 * g4 + q4;
#pragma unroll
        for (int ct = 0; ct < 4; ++ct) a2[ct] = row * 128 + (((ct ^ ((row >> 1) & 3)) << 1) | (p4 >> 1)) * 16 + (p4 & 1) * 8;
        f4 acc[4] = {};
        const u32 vg = WS_G + (cl * 4 + t4) * (56 * 1024) + lane * 16;
        v4u ga[7], gb[7], gc[7], gd[7];
        auto load7 = [&](v4u (&gfr)[7], int phi) {
            const u32 so = (u32)phi * 1024;
            gfr[0] = ld16c<0>(rsW, vg, so); gfr[1] = ld16c<0>(rsW, vg, so + 8192); gfr[2] = ld16c<0>(rsW, vg, so + 16384); gfr[3] = ld16c<0>(rsW, vg, so + 24576);
            gfr[4] = ld16c<0>(rsW, vg, so + 32768); gfr[5] = ld16c<0>(rsW, vg, so + 40960); gfr[6] = ld16c<0>(rsW, vg, so + 49152);
        };
        auto check = [&](v4u (&x)[7], v4u (&y)[7]) {
            u32 m0 = 0, m1 = 0;
#pragma unroll
            for (int p = 0; p < 7; ++p) { m0 = mx4(m0, x[p]); m1 = mx4(m1, y[p]); }
            return !__any(mx2(m0, m1) == EMPTY);
        };
        auto fetch = [&](v4u (&x)[7], v4u (&y)[7], int phi) {
#pragma unroll 1
            for (u32 sp = 0; sp < SPIN_CAP; ++sp) {
                load7(x, phi); load7(y, phi + 2);
                WAITV(0);
                PIN7(x); PIN7(y);
                if (check(x, y)) break;
            }
        };
        auto quarter = [&](v4u (&gfr)[7], int slot) {
            u32 a[4];
#pragma unroll
            for (int ct = 0; ct < 4; ++ct) a[ct] = a2[ct] + (u32)slot * SLOT;
            bf8 Aw[3][4];
#pragma unroll
            for (int p = 0; p < 2; ++p)
#pragma unroll
                for (int ct = 0; ct < 4; ++ct) Aw[p][ct] = trfrag(a[ct] + p * 4096, a[ct] + p * 4096 + 2048);
            SB();
#pragma unroll
            for (int p = 0; p < 7; ++p) {
#pragma unroll
                for (int ct = 0; ct < 4; ++ct) {
                    if (p + 2 < 7) Aw[(p + 2) % 3][ct] = trfrag(a[ct] + (p + 2) * 4096, a[ct] + (p + 2) * 4096 + 2048);
                    acc[ct] = mfma16(Aw[p % 3][ct], asbf(gfr[p]), acc[ct]);
                    SB();
                }
            }
        };
        {
            const int l7 = lane < 7 ? lane : (lane < 14 ? lane - 7 : 0);
            poll_written(rsW, WS_G + ((cl * 4 + t4) * 56 + l7 * 8 + wh + (lane >= 7 && lane < 14 ? 2 : 0)) * 1024 + 1016);
        }
        STAMP(9);
        fetch(ga, gb, wh);
        load7(gc, 4 + wh); load7(gd, 6 + wh);
        STAMP(10);
        quarter(ga, wh ? 3 : 2);
        BAR();
        STAMP(28);
        w2_run(wh ? 6 : 5, wh ? 3 : 2);
        quarter(gb, wh ? 0 : 4);
        STAMP(29);
        WAITV(0);
        PIN7(gc); PIN7(gd);
        if (!check(gc, gd)) fetch(gc, gd, 4 + wh);
        BAR();
        STAMP(11);
        if (wh) w2_run(7, 4);
        quarter(gc, wh ? 2 : 1);
        WAITV(0);
        BAR();
        quarter(gd, wh ? 4 : 3);
        STAMP(12);
        f4 keep0, keep1, send0, send1;
        if (wh == 0) {
            keep0 = acc[0] + (f4){blo(hp[0].x), bhi(hp[0].x), blo(hp[0].y), bhi(hp[0].y)};
            keep1 = acc[1] + (f4){blo(hp[1].x), bhi(hp[1].x), blo(hp[1].y), bhi(hp[1].y)};
            send0 = acc[2] + (f4){blo(hp[2].x), bhi(hp[2].x), blo(hp[2].y), bhi(hp[2].y)};
            send1 = acc[3] + (f4){blo(hp[3].x), bhi(hp[3].x), blo(hp[3].y), bhi(hp[3].y)};
        } else { keep0 = acc[2]; keep1 = acc[3]; send0 = acc[0]; send1 = acc[1]; }
        *(LAS f4*)(size_t)(L_RED + w * 2048 + lane * 16) = send0;
        *(LAS f4*)(size_t)(L_RED + w * 2048 + 1024 + lane * 16) = send1;
        BAR();
        keep0 += *(LAS f4*)(size_t)(L_RED + (w ^ 4) * 2048 + lane * 16) + *(LAS f4*)(size_t)(L_B2 + (32 * wh + 4 * g4) * 4);
        keep1 += *(LAS f4*)(size_t)(L_RED + (w ^ 4) * 2048 + 1024 + lane * 16) + *(LAS f4*)(size_t)(L_B2 + (32 * wh + 16 + 4 * g4) * 4);
        const u32 a0 = pk2(keep0[0], keep0[1]), a1 = pk2(keep0[2], keep0[3]), c0 = pk2(keep1[0], keep1[1]), c1 = pk2(keep1[2], keep1[3]);
        const auto s0 = __builtin_amdgcn_permlane16_swap(a0, c0, false, false);
        const auto s1 = __builtin_amdgcn_permlane16_swap(a1, c1, false, false);
        const v4u o = {s0[0], s1[0], s0[1], s1[1]};
        *(GAS v4u*)((char*)outp + (size_t)(r0 + 16 * t4 + i16) * 896 + j * 128 + wh * 64 + (g4 & 1) * 32 + (g4 >> 1) * 16) = o;
        STAMP(13); STAMPC(31);
        STAMPS_OUT();
    }
}

extern "C" void kernel_launch(void* const* d_in, const int* in_sizes, int n_in, void* d_out, int out_size, void* d_ws, size_t ws_size,
                              hipStream_t stream) {
    (void)in_sizes; (void)n_in; (void)out_size; (void)ws_size;
    (void)hipFuncSetAttribute((const void*)fused_block, hipFuncAttributeMaxDynamicSharedMemorySize, LDS_BYTES);
    hipLaunchKernelGGL(fused_block, dim3(256), dim3(512), LDS_BYTES, stream, (const u16*)d_in[0], (const u16*)d_in[1], (const u16*)d_in[2],
                       (const u16*)d_in[4], (const u16*)d_in[5], (const u16*)d_in[6], (const u16*)d_in[7], (u16*)d_out, (char*)d_ws);
}
```

```cpp
#include <hip/hip_runtime.h>
#include <hip/hip_bf16.h>
#include <stdint.h>

typedef unsigned short u16;
typedef unsigned int u32;
typedef unsigned long long u64;
typedef u32 v4u __attribute__((ext_vector_type(4)));
typedef u32 v2u __attribute__((ext_vector_type(2)));
typedef float f2 __attribute__((ext_vector_type(2)));
typedef float f4 __attribute__((ext_vector_type(4)));
typedef float f16v __attribute__((ext_vector_type(16)));
typedef __bf16 bf8 __attribute__((ext_vector_type(8)));
typedef __bf16 bf2 __attribute__((ext_vector_type(2)));
typedef short s4 __attribute__((ext_vector_type(4)));
typedef short s8 __attribute__((ext_vector_type(8)));
typedef __amdgpu_buffer_rsrc_t rsrc_t;
#define LAS __attribute__((address_space(3)))
#define GAS __attribute__((address_space(1)))
#define DI __device__ __forceinline__
template <int N> struct IC { static constexpr int value = N; };

#define WARM 1
#define PSLEEP 2
#define GMARK 1
#define PREFETCH 0
#define ROTA 1
#define DOT2 1
#define SLEEP_O 8
#define SLEEP_H 6
#define SLEEP_GA 4
#define SLEEP_GB 0
#define SLEEP_MARK 32
#define HFIRST 0
#define GROT 0
#define SLEEP_B 12
#define JMAJOR 1
#define CODEPF 1
constexpr int S_ = 2048, D_ = 448, F_ = 1792;
constexpr u32 FL = 128;
constexpr u32 WS_GEN = 0;
constexpr u32 WS_FP = 32 * FL;
constexpr u32 WS_FG = WS_FP + 64 * FL;
constexpr u32 WS_O = 65536;
constexpr u32 HS_BYTES = 61440;
constexpr u32 WS_H = WS_O + 32 * 65536;
constexpr u32 WS_G = WS_H + 32 * HS_BYTES;
constexpr u32 WS_STAMP = WS_G + 32 * 229376;
constexpr u32 WS_BYTES = WS_STAMP + 256 * 8 * 32 * 8;
constexpr u32 EMPTY = 0xffffffffu;
constexpr u32 OOB = 0x7fffff00u;
constexpr int SLOT = 28672;
constexpr int L_RING = 0, RING_SLOT = 22528;
constexpr int L_XN = 90112;
constexpr int L_QKV = 114688, QKV_RS = 400;
constexpr int L_WO = 0;
constexpr int L_STA = 57344;
constexpr int L_STB = 0;
constexpr int L_STC = 57344;
constexpr int CODE_PF_BYTES = 35840;
constexpr int L_HN = 0;
constexpr int L_RED = 143360;
constexpr int L_CTL = 161792;
constexpr int L_B1 = L_CTL, L_B2 = L_CTL + 1024, L_EP = L_CTL + 1280;
constexpr int L_CNT = L_CTL + 1296;
constexpr int CN_M = 0, CN_S1 = 1, CN_S2 = 2, CN_S3 = 3, CN_S4 = 4, CN_S5 = 5, CN_H = 6, CN_P = 7, CN_R = 8, CN_R2 = 9;
constexpr int L_DUMMY = L_CTL + 1536;
constexpr int LDS_BYTES = 163840;
constexpr u32 SPIN_CAP = 1u << 15;

DI u32 pk2(float lo, float hi) { f2 v = {lo, hi}; return __builtin_bit_cast(u32, __builtin_convertvector(v, bf2)); }
DI float blo(u32 x) { return __uint_as_float(x << 16); }
DI float bhi(u32 x) { return __uint_as_float(x & 0xffff0000u); }
template <int CTRL> DI float dpp(float x) { return __builtin_bit_cast(float, __builtin_amdgcn_mov_dpp(__builtin_bit_cast(int, x), CTRL, 0xf, 0xf, true)); }
DI float sum8(float x) { x += dpp<0xB1>(x); x += dpp<0x4E>(x); x += dpp<0x141>(x); return x; }
DI float xsum16(float x) { auto s = __builtin_amdgcn_permlane16_swap(__float_as_uint(x), __float_as_uint(x), false, false); return __uint_as_float(s[0]) + __uint_as_float(s[1]); }
DI float xsum32(float x) { auto s = __builtin_amdgcn_permlane32_swap(__float_as_uint(x), __float_as_uint(x), false, false); return __uint_as_float(s[0]) + __uint_as_float(s[1]); }
DI float xmax16(float x) { auto s = __builtin_amdgcn_permlane16_swap(__float_as_uint(x), __float_as_uint(x), false, false); return fmaxf(__uint_as_float(s[0]), __uint_as_float(s[1])); }
DI float xmax32(float x) { auto s = __builtin_amdgcn_permlane32_swap(__float_as_uint(x), __float_as_uint(x), false, false); return fmaxf(__uint_as_float(s[0]), __uint_as_float(s[1])); }

DI bf8 trfrag(u32 a0, u32 a1) {
    s4 x = __builtin_amdgcn_ds_read_tr16_b64_v4i16((LAS s4*)(size_t)a0);
    s4 y = __builtin_amdgcn_ds_read_tr16_b64_v4i16((LAS s4*)(size_t)a1);
    s8 r = __builtin_shufflevector(x, y, 0, 1, 2, 3, 4, 5, 6, 7);
    return __builtin_bit_cast(bf8, r);
}
DI bf8 lds128(u32 a) { return __builtin_bit_cast(bf8, *(LAS v4u*)(size_t)a); }
DI bf8 asbf(v4u x) { return __builtin_bit_cast(bf8, x); }
DI f4 mfma16(bf8 a, bf8 b, f4 c) { return __builtin_amdgcn_mfma_f32_16x16x32_bf16(a, b, c, 0, 0, 0); }

#define BAR() do { asm volatile("" ::: "memory"); __builtin_amdgcn_s_waitcnt(0xC07F); __builtin_amdgcn_s_barrier(); asm volatile("" ::: "memory"); } while (0)
#define WAITV(n) asm volatile("s_waitcnt vmcnt(" #n ")" ::: "memory")
#define SB() __builtin_amdgcn_sched_barrier(0)

DI void dma16(rsrc_t rs, u32 voff, u32 soff, u32 lds) {
    asm volatile("s_mov_b32 m0, %0\n\ts_nop 0\n\tbuffer_load_dwordx4 %1, %2, %3 offen lds" ::"s"(lds), "v"(voff), "s"(rs), "s"(soff) : "memory");
}
template <int N> DI void dma_run(rsrc_t rs, u32 voff, u32 soff, u32 sstep, u32 lds, u32 lstep) {
    u32 t;
    asm volatile("s_mov_b32 m0, %3\n\ts_mov_b32 %0, %2\n\t.rept %7\n\tbuffer_load_dwordx4 %1, %4, %0 offen lds\n\ts_add_u32 m0, m0, %6\n\ts_add_u32 %0, %0, %5\n\t.endr"
                 : "=&s"(t) : "v"(voff), "s"(soff), "s"(lds), "s"(rs), "s"(sstep), "s"(lstep), "i"(N) : "memory", "scc");
}
DI void dma_run7(rsrc_t rs, u32 voff, u32 s0, u32 sstep, u32 p0, u32 lds, u32 lstep) {
    u32 so = s0 + p0 * sstep, t;
    const u32 lim = s0 + 7 * sstep, wr = 7 * sstep;
    asm volatile("s_mov_b32 m0, %[lds]\n\ts_nop 0\n\t.rept 7\n\tbuffer_load_dwordx4 %[v], %[rs], %[so] offen lds\n\ts_add_u32 m0, m0, %[ls]\n\t"
                 "s_add_u32 %[so], %[so], %[st]\n\ts_cmp_ge_u32 %[so], %[lim]\n\ts_cselect_b32 %[t], %[wr], 0\n\ts_sub_u32 %[so], %[so], %[t]\n\t.endr"
                 : [so] "+s"(so), [t] "=&s"(t)
                 : [v] "v"(voff), [rs] "s"(rs), [lds] "s"(lds), [ls] "s"(lstep), [st] "s"(sstep), [lim] "s"(lim), [wr] "s"(wr)
                 : "memory", "scc");
}
DI void touch4(rsrc_t rs, u32 voff, u32 lds) {
    asm volatile("s_mov_b32 m0, %0\n\ts_nop 0\n\tbuffer_load_dword %1, %2, 0 offen lds" ::"s"(lds), "v"(voff), "s"(rs) : "memory");
}
template <int OFF> DI v4u ld16(rsrc_t rs, u32 voff, u32 soff) {
    v4u r; asm volatile("buffer_load_dwordx4 %0, %1, %2, %3 offen offset:%4" : "=v"(r) : "v"(voff), "s"(rs), "s"(soff), "i"(OFF) : "memory"); return r;
}
template <int OFF> DI v4u ld16c(rsrc_t rs, u32 voff, u32 soff) {
    v4u r; asm volatile("buffer_load_dwordx4 %0, %1, %2, %3 offen offset:%4 sc1" : "=v"(r) : "v"(voff), "s"(rs), "s"(soff), "i"(OFF) : "memory"); return r;
}
template <int OFF> DI v2u ld8(rsrc_t rs, u32 voff, u32 soff) {
    v2u r; asm volatile("buffer_load_dwordx2 %0, %1, %2, %3 offen offset:%4" : "=v"(r) : "v"(voff), "s"(rs), "s"(soff), "i"(OFF) : "memory"); return r;
}
template <int OFF> DI v2u ld8c(rsrc_t rs, u32 voff, u32 soff) {
    v2u r; asm volatile("buffer_load_dwordx2 %0, %1, %2, %3 offen offset:%4 sc1" : "=v"(r) : "v"(voff), "s"(rs), "s"(soff), "i"(OFF) : "memory"); return r;
}
DI void st16c(rsrc_t rs, u32 voff, v4u v) { __builtin_amdgcn_raw_buffer_store_b128(v, rs, voff, 0, 16); }
DI void st8c(rsrc_t rs, u32 voff, v2u v) { __builtin_amdgcn_raw_buffer_store_b64(v, rs, voff, 0, 16); }
#define PIN1(a) asm volatile("" : "+v"(a))
#define PIN4(a) asm volatile("" : "+v"((a)[0]), "+v"((a)[1]), "+v"((a)[2]), "+v"((a)[3]))
#define PIN7(a) asm volatile("" : "+v"((a)[0]), "+v"((a)[1]), "+v"((a)[2]), "+v"((a)[3]), "+v"((a)[4]), "+v"((a)[5]), "+v"((a)[6]))

DI u32 nz(u32 x) { return x < EMPTY - 1 ? x : EMPTY - 1; }
DI v4u nz4(v4u v) { return (v4u){nz(v.x), nz(v.y), nz(v.z), nz(v.w)}; }
DI u32 mx4(u32 m, v4u v) { const u32 a = m > v.x ? m : v.x, b = a > v.y ? a : v.y, c = b > v.z ? b : v.z; return c > v.w ? c : v.w; }
DI void poll_written(rsrc_t rs, u32 voff) {
#pragma unroll 1
    for (u32 sp = 0; sp < SPIN_CAP; ++sp) {
        v2u f;
        asm volatile("buffer_load_dwordx2 %0, %1, %2, 0 offen sc1\n\ts_waitcnt vmcnt(0)" : "=&v"(f) : "v"(voff), "s"(rs) : "memory");
        if (!__any(f.x == EMPTY || f.y == EMPTY)) break;
        __builtin_amdgcn_s_sleep(PSLEEP);
    }
}
DI u32 mx2(u32 a, u32 b) { return a > b ? a : b; }

DI bool has_empty(u32 m) { return __any(m == EMPTY); }
DI void refetch4(v4u& a, v4u& b, v4u& c, v4u& d, rsrc_t rs, u32 v, u32 so) {
#pragma unroll 1
    for (u32 sp = 0; sp < SPIN_CAP; ++sp) {
        asm volatile("; REFETCH\n\tbuffer_load_dwordx4 %0, %4, %5, %6 offen sc1\n\tbuffer_load_dwordx4 %1, %4, %5, %6 offen offset:1024 sc1\n\t"
                     "buffer_load_dwordx4 %2, %4, %5, %6 offen offset:2048 sc1\n\tbuffer_load_dwordx4 %3, %4, %5, %6 offen offset:3072 sc1\n\ts_waitcnt vmcnt(0)"
                     : "+v"(a), "+v"(b), "+v"(c), "+v"(d) : "v"(v), "s"(rs), "s"(so) : "memory");
        if (!has_empty(mx2(mx4(mx4(0u, a), b), mx4(mx4(0u, c), d)))) break;
    }
}
DI void refetch2(v4u& a, v4u& b, rsrc_t rs, u32 v, u32 so) {
#pragma unroll 1
    for (u32 sp = 0; sp < SPIN_CAP; ++sp) {
        asm volatile("; REFETCH\n\tbuffer_load_dwordx4 %0, %2, %3, %4 offen sc1\n\tbuffer_load_dwordx4 %1, %2, %3, %4 offen offset:1024 sc1\n\ts_waitcnt vmcnt(0)"
                     : "+v"(a), "+v"(b) : "v"(v), "s"(rs), "s"(so) : "memory");
        if (!has_empty(mx4(mx4(0u, a), b))) break;
    }
}
DI void refetch_st(v2u& a, v2u& b, rsrc_t rs, u32 v0, u32 v1) {
#pragma unroll 1
    for (u32 sp = 0; sp < SPIN_CAP; ++sp) {
        asm volatile("; REFETCH\n\tbuffer_load_dwordx2 %0, %2, %4, 0 offen sc1\n\tbuffer_load_dwordx2 %1, %3, %4, 0 offen sc1\n\ts_waitcnt vmcnt(0)"
                     : "+v"(a), "+v"(b) : "v"(v0), "v"(v1), "s"(rs) : "memory");
        if (!has_empty(mx4(0u, (v4u){a.x, a.y, b.x, b.y}))) break;
    }
}
DI void refetch1(v4u& a, rsrc_t rs, u32 v, u32 so) {
#pragma unroll 1
    for (u32 sp = 0; sp < SPIN_CAP; ++sp) {
        asm volatile("; REFETCH\n\tbuffer_load_dwordx4 %0, %1, %2, %3 offen sc1\n\ts_nop 0\n\ts_waitcnt vmcnt(0)" : "+v"(a) : "v"(v), "s"(rs), "s"(so) : "memory");
        if (!has_empty(mx4(0u, a))) break;
    }
}
template <int N> DI void waitv() { asm volatile("s_waitcnt vmcnt(%0)" ::"i"(N) : "memory"); }
DI u32 mx7(v4u (&x)[7]) { return mx2(mx4(mx4(mx4(mx4(0u, x[0]), x[1]), x[2]), x[3]), mx4(mx4(mx4(0u, x[4]), x[5]), x[6])); }
DI void refetch7(v4u (&x)[7], rsrc_t rs, u32 v, u32 s0) {
#pragma unroll 1
    for (u32 sp = 0; sp < SPIN_CAP; ++sp) {
        u32 so = s0;
        asm volatile("; REFETCH\n\t.irp r, %0, %1, %2, %3, %4, %5, %6\n\tbuffer_load_dwordx4 \\r, %8, %9, %7 offen sc1\n\ts_add_u32 %7, %7, 0x2000\n\t.endr\n\ts_waitcnt vmcnt(0)"
                     : "+v"(x[0]), "+v"(x[1]), "+v"(x[2]), "+v"(x[3]), "+v"(x[4]), "+v"(x[5]), "+v"(x[6]), "+s"(so) : "v"(v), "s"(rs) : "memory", "scc");
        if (!has_empty(mx7(x))) break;
    }
}
DI void cn_signal(int cn, int lane) {
    asm volatile("" ::: "memory");
    __builtin_amdgcn_s_waitcnt(0xC07F);
    if (lane == 0) __hip_atomic_fetch_add((LAS u32*)(size_t)(L_CNT + 4 * cn), 1u, __ATOMIC_RELAXED, __HIP_MEMORY_SCOPE_WORKGROUP);
    asm volatile("" ::: "memory");
}
DI void cn_await(int cn, u32 n) {
#pragma unroll 1
    for (u32 sp = 0; sp < SPIN_CAP * 16; ++sp) {
        asm volatile("" ::: "memory");
        if ((u32)__builtin_amdgcn_readfirstlane(*(volatile LAS u32*)(size_t)(L_CNT + 4 * cn)) >= n) break;
        __builtin_amdgcn_s_sleep(1);
    }
    asm volatile("" ::: "memory");
}
DI void poll_flags(rsrc_t rs, u32 voff, int nflags, u64 ep, int lane) {
    const bool act = lane < nflags;
    voff = act ? voff : OOB;
    const u32 elo = (u32)ep, ehi = (u32)(ep >> 32);
#pragma unroll 1
    for (u32 sp = 0; sp < SPIN_CAP; ++sp) {
        v2u f;
        asm volatile("buffer_load_dwordx2 %0, %1, %2, 0 offen sc1\n\ts_waitcnt vmcnt(0)" : "=&v"(f) : "v"(voff), "s"(rs) : "memory");
        const bool ok = !act || (f.x == elo && f.y == ehi);
        if (__all(ok)) break;
        __builtin_amdgcn_s_sleep(1);
    }
}
DI void put_flag(rsrc_t rs, u32 off, u64 ep, int lane) {
    if (lane == 0) st8c(rs, off, (v2u){(u32)ep, (u32)(ep >> 32)});
}

#define STAMP(k) asm volatile("; MARK " #k)
#define STAMPP(k) do { } while (0)
#define STAMPC(k) do { } while (0)
#define STAMPS_DECL do { } while (0)
#define STAMPS_OUT() do { } while (0)

DI void ln1_group(v4u (&xv)[7], int grp, int lane) {
    f2 v[28];
    const bf2 one = {(__bf16)1.0f, (__bf16)1.0f};
    float s[4] = {0.f, 0.f, 0.f, 0.f}, q[4] = {0.f, 0.f, 0.f, 0.f};
#pragma unroll
    for (int c = 0; c < 7; ++c)
#pragma unroll
        for (int e = 0; e < 4; ++e) {
            const u32 xe = xv[c][e];
            const bf2 x = __builtin_bit_cast(bf2, xe);
            s[e] = __builtin_amdgcn_fdot2_f32_bf16(x, one, s[e], false);
            q[e] = __builtin_amdgcn_fdot2_f32_bf16(x, x, q[e], false);
        }
    const float mean = sum8((s[0] + s[1]) + (s[2] + s[3])) * (1.0f / 448.0f);
    const float var = fmaxf(sum8((q[0] + q[1]) + (q[2] + q[3])) * (1.0f / 448.0f) - mean * mean, 0.f);
#pragma unroll
    for (int c = 0; c < 7; ++c)
#pragma unroll
        for (int e = 0; e < 4; ++e) v[4 * c + e] = (f2){blo(xv[c][e]), bhi(xv[c][e])};
    const float rstd = __builtin_amdgcn_rsqf(var + 1e-5f), nb = -mean * rstd;
    const f2 r2 = {rstd, rstd}, n2 = {nb, nb};
    const int gi = lane & 7, R = 8 * grp + (lane >> 3), gp = gi & 3;
    const u32 a = L_XN + ((R >> 4) * 14 + (gi >> 2)) * 1024 + (2 * (gp & 1) * 16 + ((R & 15) ^ (2 * (2 * (gi >> 2) + (gp & 1))))) * 16 + (gp >> 1) * 8;
#pragma unroll
    for (int c = 0; c < 7; ++c) {
        const f2 y0 = v[4 * c] * r2 + n2, y1 = v[4 * c + 1] * r2 + n2, y2 = v[4 * c + 2] * r2 + n2, y3 = v[4 * c + 3] * r2 + n2;
        *(LAS v2u*)(size_t)(a + c * 2048) = (v2u){pk2(y0.x, y0.y), pk2(y1.x, y1.y)};
        *(LAS v2u*)(size_t)(a + c * 2048 + 256) = (v2u){pk2(y2.x, y2.y), pk2(y3.x, y3.y)};
    }
}

DI v2u gelu4_pk(float u0, float u1, float u2, float u3, float k1) {
    float a, b, c, d; v2u r;
    asm("v_mul_f32 %0, %6, %6\n\tv_mul_f32 %1, %7, %7\n\tv_mul_f32 %2, %8, %8\n\tv_mul_f32 %3, %9, %9\n\t"
        "v_fmaak_f32 %0, %0, %10, 0xc0135761\n\tv_fmaak_f32 %1, %1, %10, 0xc0135761\n\tv_fmaak_f32 %2, %2, %10, 0xc0135761\n\tv_fmaak_f32 %3, %3, %10, 0xc0135761\n\t"
        "v_mul_f32 %0, %6, %0\n\tv_mul_f32 %1, %7, %1\n\tv_mul_f32 %2, %8, %2\n\tv_mul_f32 %3, %9, %3\n\t"
        "v_exp_f32 %0, %0\n\tv_exp_f32 %1, %1\n\tv_exp_f32 %2, %2\n\tv_exp_f32 %3, %3\n\t"
        "v_add_f32 %0, 1.0, %0\n\tv_add_f32 %1, 1.0, %1\n\tv_add_f32 %2, 1.0, %2\n\tv_add_f32 %3, 1.0, %3\n\t"
        "v_rcp_f32 %0, %0\n\tv_rcp_f32 %1, %1\n\tv_rcp_f32 %2, %2\n\tv_rcp_f32 %3, %3\n\t"
        "v_mul_f32 %0, %6, %0\n\tv_mul_f32 %1, %7, %1\n\tv_mul_f32 %2, %8, %2\n\tv_mul_f32 %3, %9, %3\n\t"
        "v_cvt_pk_bf16_f32 %4, %0, %1\n\tv_cvt_pk_bf16_f32 %5, %2, %3"
        : "=&v"(a), "=&v"(b), "=&v"(c), "=&v"(d), "=&v"(r.x), "=&v"(r.y)
        : "v"(u0), "v"(u1), "v"(u2), "v"(u3), "v"(k1));
    return r;
}

__global__ void __launch_bounds__(512, 2)
fused_block(const u16* __restrict__ xp, const u16* __restrict__ wqkvp, const u16* __restrict__ wop, const u16* __restrict__ w1p,
            const u16* __restrict__ b1p, const u16* __restrict__ w2p, const u16* __restrict__ b2p, u16* __restrict__ outp, char* __restrict__ wsp) {
    u64 pc; asm volatile("s_getpc_b64 %0" : "=s"(pc));
    asm volatile("" ::"s"(xp), "s"(wqkvp), "s"(wop), "s"(w1p), "s"(b1p), "s"(w2p), "s"(b2p), "s"(outp), "s"(wsp));
    const int tid = threadIdx.x, lane = tid & 63;
    const int w = __builtin_amdgcn_readfirstlane(tid >> 6);
    const int b = blockIdx.x, xi = b >> 3, c = JMAJOR ? (xi & 3) : (xi >> 3), j = JMAJOR ? (xi >> 2) : (xi & 7), cl = (b & 7) * 4 + c, r0 = cl * 64;
    if (xi < 5 && w == 7) {
        const rsrc_t rsC = __builtin_amdgcn_make_buffer_rsrc((void*)(pc & ~127ull), 0, CODE_PF_BYTES, 0x00020000);
        touch4(rsC, (u32)(xi * 64 + lane) * 128, L_DUMMY);
    }
    const rsrc_t rsX = __builtin_amdgcn_make_buffer_rsrc((void*)xp, 0, S_ * D_ * 2, 0x00020000);
    const rsrc_t rsW = __builtin_amdgcn_make_buffer_rsrc((void*)wsp, 0, WS_BYTES, 0x00020000);
    STAMPS_DECL;
    STAMP(0); STAMPC(30);
    const int g4 = lane >> 4, q4 = (lane & 15) >> 2, p4 = lane & 3, i16 = lane & 15;
    const rsrc_t rsO = __builtin_amdgcn_make_buffer_rsrc((void*)wop, 0, D_ * D_ * 2, 0x00020000);
    const rsrc_t rs1 = __builtin_amdgcn_make_buffer_rsrc((void*)w1p, 0, D_ * F_ * 2, 0x00020000);
    const u32 swz = (u32)(((lane & 7) ^ (((lane >> 4) & 3) << 1)) * 16);
    const u32 vw1 = (u32)((lane >> 3) * 3584 + j * 512) + swz;
    auto w1_pieces = [&](auto npc, int u, int pc0, int lds) {
        dma_run<decltype(npc)::value>(rs1, vw1, (u32)(pc0 * 8 * 3584 + u * 128), 8 * 3584, (u32)lds, 1024);
    };
    const v4u empty4 = {EMPTY, EMPTY, EMPTY, EMPTY};
    const int k0 = (2 * c + (j & 1)) % 7;

    if (w < 6) {
        v4u xa[7], xb[7];
        const int ra = r0 - 16 + 8 * w + (lane >> 3);
        const u32 va = (u32)(ra < 0 ? 0 : ra) * 896 + (lane & 7) * 16;
        xa[0] = ld16<0>(rsX, va, 0); xa[1] = ld16<128>(rsX, va, 0); xa[2] = ld16<256>(rsX, va, 0); xa[3] = ld16<384>(rsX, va, 0);
        xa[4] = ld16<512>(rsX, va, 0); xa[5] = ld16<640>(rsX, va, 0); xa[6] = ld16<768>(rsX, va, 0);
        if (w < 4) {
            const u32 vb = (u32)(r0 + 32 + 8 * w + (lane >> 3)) * 896 + (lane & 7) * 16;
            xb[0] = ld16<0>(rsX, vb, 0); xb[1] = ld16<128>(rsX, vb, 0); xb[2] = ld16<256>(rsX, vb, 0); xb[3] = ld16<384>(rsX, vb, 0);
            xb[4] = ld16<512>(rsX, vb, 0); xb[5] = ld16<640>(rsX, vb, 0); xb[6] = ld16<768>(rsX, vb, 0);
            STAMP(14);
            WAITV(7);
        } else {
            STAMP(14);
            WAITV(0);
        }
        STAMP(15);
        PIN7(xa);
        ln1_group(xa, w, lane);
        if (w < 4) { WAITV(0); PIN7(xb); ln1_group(xb, 6 + w, lane); }
        STAMP(1);
        auto p1_loop = [&](auto ncc, auto ntc, const int c0, const int T0) {
            constexpr int NC = decltype(ncc)::value, NT = decltype(ntc)::value;
            const u32 abase = L_RING + (4 * g4 + q4) * 352 + p4 * 8 + 32 * c0;
            u32 bb[2];
#pragma unroll
            for (int s = 0; s < 2; ++s) bb[s] = L_XN + T0 * 14336 + s * 1024 + (g4 * 16 + (i16 ^ (2 * (2 * s + (g4 >> 1))))) * 16;
            f4 acc[NC][NT] = {};
            bf8 Ap[NC] = {}, Bp[NT] = {};
            int kc = k0;
#pragma unroll 1
            for (int i = 0; i < 7; ++i) {
                BAR();
                STAMPP(17 + i);
                const u32 aa = abase + ((i + 1) & 3) * RING_SLOT, bo = kc * 2048;
                kc = kc == 6 ? 0 : kc + 1;
                bf8 A[NC], B[NT];
#pragma unroll
                for (int n = 0; n < NC * NT; ++n) {
                    acc[n / NT][n % NT] = mfma16(Ap[n / NT], Bp[n % NT], acc[n / NT][n % NT]);
                    if (n == 0) A[0] = trfrag(aa, aa + 5632);
                    else if (n <= NT) B[n - 1] = lds128(bb[0] + bo + (n - 1) * 14336);
                    else if (n < NC + NT) A[n - NT] = trfrag(aa + 32 * (n - NT), aa + 32 * (n - NT) + 5632);
                    SB();
                }
#pragma unroll
                for (int n = 0; n < NC * NT; ++n) {
                    acc[n / NT][n % NT] = mfma16(A[n / NT], B[n % NT], acc[n / NT][n % NT]);
                    if (n == 0) Ap[0] = trfrag(aa + 11264, aa + 11264 + 5632);
                    else if (n <= NT) Bp[n - 1] = lds128(bb[1] + bo + (n - 1) * 14336);
                    else if (n < NC + NT) Ap[n - NT] = trfrag(aa + 11264 + 32 * (n - NT), aa + 11264 + 32 * (n - NT) + 5632);
                    SB();
                }
            }
#pragma unroll
            for (int n = 0; n < NC * NT; ++n) acc[n / NT][n % NT] = mfma16(Ap[n / NT], Bp[n % NT], acc[n / NT][n % NT]);
            BAR();
            STAMP(2);
#pragma unroll
            for (int cc = 0; cc < NC; ++cc) {
                const int col = 16 * (c0 + cc) + 4 * g4, part = col >= 112 ? 2 : (col >= 56 ? 1 : 0), rem = col - 56 * part, head = rem >= 28 ? 1 : 0;
                const u32 a = L_QKV + (16 * T0 + i16) * QKV_RS + (2 * part + head) * 64 + (rem - 28 * head) * 2;
                if (col < 168) {
#pragma unroll
                    for (int t = 0; t < NT; ++t)
                        *(LAS v2u*)(size_t)(a + t * 16 * QKV_RS) = (v2u){pk2(acc[cc][t][0], acc[cc][t][1]), pk2(acc[cc][t][2], acc[cc][t][3])};
                }
            }
        };
        if (w == 2 || w == 3) p1_loop(IC<3>{}, IC<5>{}, 3 * (w - 1), 0);
        else if (w == 0 || w == 4) p1_loop(IC<3>{}, IC<2>{}, 0, 1 + (w >> 1));
        else if (w == 1) p1_loop(IC<2>{}, IC<3>{}, 9, 0);
        else p1_loop(IC<2>{}, IC<2>{}, 9, 3);
    } else {
        const rsrc_t rsQ = __builtin_amdgcn_make_buffer_rsrc((void*)wqkvp, 0, D_ * 3 * D_ * 2, 0x00020000);
        const int lw = w - 6;
        u32 vq[11];
#pragma unroll
        for (int i = 0; i < 11; ++i) {
            const int m = 64 * (lw + 2 * i) + lane, row = m / 22, gr = m - row * 22;
            const int part = gr < 7 ? 0 : (gr < 14 ? 1 : 2); int sub = gr - 7 * part; sub = sub > 6 ? 6 : sub;
            vq[i] = (u32)(row * 2688 + part * 896 + j * 112 + sub * 16);
        }
        auto issue_chunk = [&](int i) {
            int kc = k0 + i; kc = kc >= 7 ? kc - 7 : kc;
            const u32 so = (u32)kc * (64 * 2688), ld = L_RING + ((i + 1) & 3) * RING_SLOT + lw * 1024;
#pragma unroll
            for (int p = 0; p < 11; ++p) dma16(rsQ, vq[p], so, ld + p * 2048);
        };
        v2u gen = {0u, 0u}, b1v = {0u, 0u}, b2v = {0u, 0u};
        if (lw == 1) gen = ld8c<0>(rsW, WS_GEN + cl * FL, 0);
        else {
            const rsrc_t rsb1 = __builtin_amdgcn_make_buffer_rsrc((void*)b1p, 0, F_ * 2, 0x00020000);
            const rsrc_t rsb2 = __builtin_amdgcn_make_buffer_rsrc((void*)b2p, 0, D_ * 2, 0x00020000);
            b1v = ld8<0>(rsb1, j * 512 + lane * 8, 0);
            b2v = ld8<0>(rsb2, j * 128 + (lane & 15) * 8, 0);
        }
        const bool marker = j == 7 || j == 0;
        if (marker) {
            const u32 mk0 = (j == 7 ? WS_O + cl * 65536 + lw * 32768 : WS_H + cl * HS_BYTES + lw * 30720) + lane * 16;
            const int mkn = j == 7 ? 32 : 30;
#pragma unroll 2
            for (int p = 0; p < mkn; ++p) st16c(rsW, mk0 + p * 1024, empty4);
        }
        issue_chunk(0); issue_chunk(1);
        STAMP(14);
        WAITV(11);
        if (lw == 1) { PIN1(gen); if (lane == 0) { *(LAS u64*)(size_t)(L_EP) = (((u64)gen.y << 32) | gen.x) + 1; *(LAS v4u*)(size_t)(L_CNT) = (v4u){0u, 0u, 0u, 0u}; *(LAS v4u*)(size_t)(L_CNT + 16) = (v4u){0u, 0u, 0u, 0u}; *(LAS v4u*)(size_t)(L_CNT + 32) = (v4u){0u, 0u, 0u, 0u}; } }
        else {
            PIN1(b1v); PIN1(b2v);
            *(LAS f4*)(size_t)(L_B1 + lane * 16) = (f4){blo(b1v.x), bhi(b1v.x), blo(b1v.y), bhi(b1v.y)};
            if (lane < 16) *(LAS f4*)(size_t)(L_B2 + lane * 16) = (f4){blo(b2v.x), bhi(b2v.x), blo(b2v.y), bhi(b2v.y)};
        }
        STAMP(1);
        const u32 vo = (u32)((lane >> 3) * 896 + j * 128) + swz;
        auto wo_run = [&](auto cnt, int n0) {
            dma_run<decltype(cnt)::value>(rsO, j < 7 ? vo : OOB, (u32)n0 * 7168, 2 * 7168, (u32)(L_WO + n0 * 1024), 2048);
        };
        STAMPP(17);
        BAR();
        const u64 ep0 = *(LAS u64*)(size_t)(L_EP);
        if (marker && lw == 0 && lane == 0) st8c(rsW, WS_FP + (cl * 2 + (j == 0)) * FL, (v2u){(u32)ep0, (u32)(ep0 >> 32)});
        issue_chunk(2);
#pragma unroll 1
        for (int i = 1; i < 5; ++i) {
            WAITV(11);
            STAMPP(17 + i);
            BAR();
            issue_chunk(i + 2);
        }
        v2u fchk = ld8c<0>(rsW, lane < 2 ? WS_FP + (cl * 2 + lane) * FL : OOB, 0);
        WAITV(12); STAMPP(22); BAR();
        wo_run(IC<11>{}, lw);
        WAITV(12); STAMPP(23); BAR();
        wo_run(IC<11>{}, 22 + lw);
        wo_run(IC<6>{}, 44 + lw);
        WAITV(28); PIN1(fchk);
        if (!__all(lane >= 2 || (fchk.x == (u32)ep0 && fchk.y == (u32)(ep0 >> 32)))) poll_flags(rsW, WS_FP + (cl * 2 + lane) * FL, 2, ep0, lane);
        BAR();
        STAMP(2);
        {
            const rsrc_t rsT = __builtin_amdgcn_make_buffer_rsrc((void*)(lw == 0 ? w1p : w2p), 0, F_ * D_ * 2, 0x00020000);
            touch4(rsT, lane < 49 ? (u32)(b * 49 + lane) * 128 : OOB, L_DUMMY);
        }
#pragma unroll
        for (int n = 0; n < 4; ++n) {
            const int idx = lane + 64 * n, row = idx / 6;
            if (idx < 240) *(LAS v2u*)(size_t)(L_QKV + (40 * lw + row) * QKV_RS + (idx - 6 * row) * 64 + 56) = (v2u){0u, 0u};
        }
    }
    BAR();
    STAMP(16);
    const u64 ep = *(LAS u64*)(size_t)(L_EP);
    const int t4 = w & 3, wh = w >> 2;

    {
        const u32 rowq = L_QKV + (16 + 16 * t4 + i16) * QKV_RS + g4 * 16;
        const bf8 Qf = lds128(rowq + wh * 64);
        const bf8 K0 = lds128(rowq - 16 * QKV_RS + (2 + wh) * 64), K1 = lds128(rowq + (2 + wh) * 64);
        const u32 va = L_QKV + (16 * t4 + 4 * g4 + q4) * QKV_RS + (4 + wh) * 64 + p4 * 8;
        const bf8 V0 = trfrag(va, va + 16 * QKV_RS), V1 = trfrag(va + 32, va + 32 + 16 * QKV_RS);
        const f4 z = {0.f, 0.f, 0.f, 0.f};
        const f4 S0 = mfma16(K0, Qf, z), S1 = mfma16(K1, Qf, z);
        const float cs = 0.18898223650461362f * 1.4426950408889634f;
        float sv[8]; float mx = -3.0e38f;
        const int kbase = r0 - 16 + 16 * t4 + 4 * g4;
#pragma unroll
        for (int e = 0; e < 8; ++e) {
            const int kt = e >> 2, r = e & 3, delta = i16 + 16 - 16 * kt - 4 * g4 - r;
            const bool ok = delta >= 0 && delta <= 14 && !(delta & 1) && (kbase + 16 * kt + r) >= 0;
            sv[e] = ok ? (kt ? S1[r] : S0[r]) * cs : -3.0e38f;
            mx = fmaxf(mx, sv[e]);
        }
        mx = xmax32(xmax16(mx));
        float sum = 0.f;
#pragma unroll
        for (int e = 0; e < 8; ++e) { sv[e] = __builtin_amdgcn_exp2f(sv[e] - mx); sum += sv[e]; }
        const float inv = __builtin_amdgcn_rcpf(xsum32(xsum16(sum)));
        const v4u P = {pk2(sv[0] * inv, sv[1] * inv), pk2(sv[2] * inv, sv[3] * inv), pk2(sv[4] * inv, sv[5] * inv), pk2(sv[6] * inv, sv[7] * inv)};
        const f4 O0 = mfma16(V0, asbf(P), z), O1 = mfma16(V1, asbf(P), z);
        const v4u og = {pk2(O0[0], O0[1]), pk2(O0[2], O0[3]), pk2(O1[0], O1[1]), pk2(O1[2], O1[3])};
        st16c(rsW, WS_O + ((cl * 4 + t4) * 16 + 2 * j + wh) * 1024 + lane * 16, nz4(og));
    }
    STAMP(3);
    if (j == 7) {
        __builtin_amdgcn_s_sleep(SLEEP_MARK);
        const u32 mg = WS_G + cl * 229376 + w * 28672 + lane * 16;
#pragma unroll 4
        for (int p = 0; p < 28; ++p) st16c(rsW, mg + p * 1024, empty4);
        WAITV(0);
        BAR();
        if (w == 0 && lane == 0) st8c(rsW, WS_FG + cl * FL, (v2u){(u32)ep, (u32)(ep >> 32)});
        STAMPS_OUT();
        return;
    }
    if (w >= 6) WAITV(1);
    BAR();

    bf8 A1[14][2];
    u32 a1lane[2];
    {
        const int row = 4 * g4 + q4;
#pragma unroll
        for (int ft = 0; ft < 2; ++ft) a1lane[ft] = (u32)(row * 128 + ((((2 * (w & 1) + ft) ^ ((row >> 1) & 3)) << 1) | (p4 >> 1)) * 16 + (p4 & 1) * 8);
    }
    auto w1_read = [&](auto nkb, int kb0, int lcol) {
        constexpr int NKB = decltype(nkb)::value;
#pragma unroll
        for (int ft = 0; ft < 2; ++ft) {
            const u32 a = lcol + a1lane[ft];
#pragma unroll
            for (int r = 0; r < NKB; ++r) A1[kb0 + r][ft] = trfrag(a + r * 4096, a + r * 4096 + 2048);
        }
    };
    constexpr int LF = 57344;
    v2u hp[4];
    if (w < 4) {
        v4u xq[2];
        const u32 vx = (u32)(r0 + 16 * t4 + i16) * 896 + j * 128 + (g4 & 1) * 32 + (g4 >> 1) * 16;
        xq[0] = ld16<0>(rsX, vx, 0); xq[1] = ld16<64>(rsX, vx, 0);
        v4u of[16];
        const u32 vo = WS_O + (cl * 4 + t4) * 16384 + lane * 16;
        __builtin_amdgcn_s_sleep(SLEEP_O);
        STAMP(18);
        of[0] = ld16c<0>(rsW, vo, 0); of[1] = ld16c<1024>(rsW, vo, 0); of[2] = ld16c<2048>(rsW, vo, 0); of[3] = ld16c<3072>(rsW, vo, 0);
        of[4] = ld16c<0>(rsW, vo, 4096); of[5] = ld16c<1024>(rsW, vo, 4096); of[6] = ld16c<2048>(rsW, vo, 4096); of[7] = ld16c<3072>(rsW, vo, 4096);
        of[8] = ld16c<0>(rsW, vo, 8192); of[9] = ld16c<1024>(rsW, vo, 8192); of[10] = ld16c<2048>(rsW, vo, 8192); of[11] = ld16c<3072>(rsW, vo, 8192);
        of[12] = ld16c<0>(rsW, vo, 12288); of[13] = ld16c<1024>(rsW, vo, 12288); of[14] = ld16c<2048>(rsW, vo, 12288); of[15] = ld16c<3072>(rsW, vo, 12288);
        WAITV(16);
        PIN1(xq[0]); PIN1(xq[1]);
        v2u xr[4];
#pragma unroll
        for (int cp = 0; cp < 2; ++cp) {
            const auto s0 = __builtin_amdgcn_permlane16_swap(xq[cp].x, xq[cp].z, false, false);
            const auto s1 = __builtin_amdgcn_permlane16_swap(xq[cp].y, xq[cp].w, false, false);
            xr[2 * cp] = (v2u){s0[0], s1[0]}; xr[2 * cp + 1] = (v2u){s0[1], s1[1]};
        }
        u32 ao[2][4];
        const int rr0 = 4 * g4 + q4, rr1 = (g4 < 3 ? 16 + 4 * g4 : 24) + q4;
#pragma unroll
        for (int ct = 0; ct < 4; ++ct) {
            ao[0][ct] = L_WO + rr0 * 128 + (((ct ^ ((rr0 >> 1) & 3)) << 1) | (p4 >> 1)) * 16 + (p4 & 1) * 8;
            ao[1][ct] = L_WO + rr1 * 128 + (((ct ^ ((rr1 >> 1) & 3)) << 1) | (p4 >> 1)) * 16 + (p4 & 1) * 8;
        }
        f4 acc[4] = {};
        bf8 Aw[3][4];
        auto rdA = [&](int H, int ct) { const int cx = ct ^ ((H & 1) << 1); Aw[H % 3][ct] = trfrag(ao[0][cx] + H * 3584, ao[1][cx] + H * 3584); };
#pragma unroll
        for (int ct = 0; ct < 4; ++ct) { rdA(0, ct); rdA(1, ct); }
        SB();
#pragma unroll
        for (int G = 0; G < 4; ++G) {
            if (G == 0) WAITV(12); else if (G == 1) WAITV(8); else if (G == 2) WAITV(4); else WAITV(0);
            PIN4(&of[4 * G]);
            if (has_empty(mx2(mx4(mx4(0u, of[4 * G]), of[4 * G + 1]), mx4(mx4(0u, of[4 * G + 2]), of[4 * G + 3]))))
                refetch4(of[4 * G], of[4 * G + 1], of[4 * G + 2], of[4 * G + 3], rsW, vo, G * 4096);
            if (G == 0) STAMP(19);
            SB();
#pragma unroll
            for (int H = 4 * G; H < 4 * G + 4; ++H)
#pragma unroll
                for (int ct = 0; ct < 4; ++ct) {
                    if (H + 2 < 16) rdA(H + 2, ct);
                    acc[ct] = mfma16(Aw[H % 3][ct], asbf(of[H]), acc[ct]);
                    SB();
                }
        }
        if (j == 0 && w == 0 && lane == 0) st8c(rsW, WS_GEN + cl * FL, (v2u){(u32)ep, (u32)(ep >> 32)});
        cn_signal(CN_P, lane);
        STAMP(5);
        float hv[16];
#pragma unroll
        for (int ct = 0; ct < 4; ++ct) {
            hp[ct] = (v2u){nz(pk2(acc[ct][0] + blo(xr[ct].x), acc[ct][1] + bhi(xr[ct].x))), nz(pk2(acc[ct][2] + blo(xr[ct].y), acc[ct][3] + bhi(xr[ct].y)))};
            hv[4 * ct] = blo(hp[ct].x); hv[4 * ct + 1] = bhi(hp[ct].x); hv[4 * ct + 2] = blo(hp[ct].y); hv[4 * ct + 3] = bhi(hp[ct].y);
        }
        const u32 hb = WS_H + cl * HS_BYTES;
        const u32 vh = hb + (t4 * 14 + 2 * j) * 1024 + lane * 16;
        st16c(rsW, vh, (v4u){hp[0].x, hp[0].y, hp[1].x, hp[1].y});
        st16c(rsW, vh + 1024, (v4u){hp[2].x, hp[2].y, hp[3].x, hp[3].y});
        float s = 0.f;
#pragma unroll
        for (int e = 0; e < 16; e += 4) s += (hv[e] + hv[e + 1]) + (hv[e + 2] + hv[e + 3]);
        const float m64 = xsum32(xsum16(s)) * (1.0f / 64.0f);
        float q = 0.f, q1 = 0.f;
#pragma unroll
        for (int e = 0; e < 16; e += 2) { const float d = hv[e] - m64, d1 = hv[e + 1] - m64; q += d * d; q1 += d1 * d1; }
        q = xsum32(xsum16(q + q1));
        if (g4 == 0) st8c(rsW, hb + 57344 + j * 512 + (16 * t4 + i16) * 8, (v2u){nz(__float_as_uint(m64)), nz(__float_as_uint(q))});
        STAMP(22);
#define HLOADS st[0] = ld8c<0>(rsW, vs, 0); st[1] = ld8c<0>(rsW, vs1, 0); \
        hf[0] = ld16c<0>(rsW, vhl, 0); hf[1] = ld16c<1024>(rsW, vhl, 0); hf[2] = ld16c<2048>(rsW, vhl, 0); hf[3] = ld16c<3072>(rsW, vhl, 0); \
        hf[4] = ld16c<0>(rsW, vhl, 4096); hf[5] = ld16c<1024>(rsW, vhl, 4096); hf[6] = ld16c<2048>(rsW, vhl, 4096); hf[7] = ld16c<3072>(rsW, vhl, 4096); \
        hf[8] = ld16c<0>(rsW, vhl, 8192); hf[9] = ld16c<1024>(rsW, vhl, 8192); hf[10] = ld16c<2048>(rsW, vhl, 8192); hf[11] = ld16c<3072>(rsW, vhl, 8192); \
        hf[12] = ld16c<0>(rsW, vhl, 12288); hf[13] = ld16c<1024>(rsW, vhl, 12288);
        v4u hf[14]; v2u st[2];
        const u32 vs = hb + 57344 + (16 * t4 + i16) * 8 + 2 * g4 * 512;
        const u32 vs1 = g4 < 3 ? vs + 512 : vs;
        const u32 vhl = hb + t4 * 14336 + lane * 16;
        cn_await(CN_S2, 4);
        w1_read(IC<12>{}, 0, LF + (w >> 1) * 49152);
        cn_signal(CN_R, lane);
        STAMP(25);
        __builtin_amdgcn_s_sleep(SLEEP_H);
        STAMP(6);
        HLOADS
        cn_await(CN_S3, 4);
        w1_read(IC<2>{}, 12, (w >> 1) * 8192);
        cn_signal(CN_R2, lane);
        STAMP(23);
        cn_await(CN_R2, 4); cn_await(CN_S4, 4);
        WAITV(14); PIN1(st[0]); PIN1(st[1]);
        if (has_empty(mx4(0u, (v4u){st[0].x, st[0].y, st[1].x, st[1].y}))) refetch_st(st[0], st[1], rsW, vs, vs1);
        STAMP(24);
        const bool v1 = g4 < 3;
        const float m0 = __uint_as_float(st[0].x), m1 = __uint_as_float(st[1].x);
        const float mean = xsum32(xsum16(m0 + (v1 ? m1 : 0.f))) * (1.0f / 7.0f);
        float t = __uint_as_float(st[0].y) + 64.0f * (m0 - mean) * (m0 - mean);
        if (v1) t += __uint_as_float(st[1].y) + 64.0f * (m1 - mean) * (m1 - mean);
        const float rstd = __builtin_amdgcn_rsqf(xsum32(xsum16(t)) * (1.0f / 448.0f) + 1e-5f);
        const float nb = -mean * rstd;
        const f2 r2 = {rstd, rstd}, n2 = {nb, nb};
        const u32 ah = L_HN + t4 * 14336 + lane * 16;
#pragma unroll
        for (int G = 0; G < 4; ++G) {
            const int n = G < 3 ? 4 : 2;
            if (G == 0) WAITV(10); else if (G == 1) WAITV(6); else if (G == 2) WAITV(2); else WAITV(0);
            if (G < 3) {
                PIN4(&hf[4 * G]);
                if (has_empty(mx2(mx4(mx4(0u, hf[4 * G]), hf[4 * G + 1]), mx4(mx4(0u, hf[4 * G + 2]), hf[4 * G + 3]))))
                    refetch4(hf[4 * G], hf[4 * G + 1], hf[4 * G + 2], hf[4 * G + 3], rsW, vhl, G * 4096);
            } else {
                PIN1(hf[12]); PIN1(hf[13]);
                if (has_empty(mx4(mx4(0u, hf[12]), hf[13]))) refetch2(hf[12], hf[13], rsW, vhl, 12288);
            }
#pragma unroll
            for (int f = 4 * G; f < 4 * G + n; ++f) {
                v4u o;
#pragma unroll
                for (int e = 0; e < 4; ++e) { const f2 y = (f2){blo(hf[f][e]), bhi(hf[f][e])} * r2 + n2; o[e] = pk2(y.x, y.y); }
                *(LAS v4u*)(size_t)(ah + f * 1024) = o;
            }
        }
        cn_signal(CN_H, lane);
        STAMP(26);
    } else {
        hp[0] = hp[1] = hp[2] = hp[3] = (v2u){0u, 0u};
        const int u = w >> 1, hf2 = w & 1, cg = u - 2, la = LF + cg * 49152;
        w1_pieces(IC<24>{}, cg, 24 * hf2, la + hf2 * 24576);
        WAITV(0);
        cn_signal(CN_S2, lane);
        STAMP(20);
        cn_await(CN_P, 4);
        w1_pieces(IC<4>{}, cg, 48 + 4 * hf2, cg * 8192 + hf2 * 4096);
        w1_pieces(IC<4>{}, u, 48 + 4 * hf2, 16384 + cg * 8192 + hf2 * 4096);
        WAITV(0);
        cn_signal(CN_S3, lane); cn_await(CN_S3, 4);
        STAMP(4);
        w1_read(IC<2>{}, 12, 16384 + cg * 8192);
        cn_signal(CN_S4, lane);
        cn_await(CN_R, 4);
        __builtin_amdgcn_s_sleep(SLEEP_B);
        w1_pieces(IC<24>{}, u, 24 * hf2, la + hf2 * 24576);
        WAITV(0);
        cn_signal(CN_S1, lane); cn_await(CN_S1, 4);
        STAMP(21);
        w1_read(IC<12>{}, 0, la);
        cn_signal(CN_S5, lane); cn_await(CN_S5, 4);
        STAMP(6);
    }
    const rsrc_t rs2 = __builtin_amdgcn_make_buffer_rsrc((void*)w2p, 0, F_ * D_ * 2, 0x00020000);
    const u32 vw2 = (u32)((lane >> 3) * 896 + j * 128) + swz;
    const int pg = GROT ? (j + 2 * c) % 7 : 0;
    auto w2_run = [&](int phi, int slot) {
        dma_run7(rs2, vw2, (u32)((32 * phi + 8 * t4) * 896), 256 * 896, (u32)pg, (u32)(slot * SLOT + t4 * 1024), 4096);
    };
    auto prod = [&](int i) { return pg + i >= 7 ? pg + i - 7 : pg + i; };
    auto w2_piece = [&](int phi, int slot, int i) { dma16(rs2, vw2, (u32)((256 * prod(i) + 32 * phi + 8 * t4) * 896), (u32)(slot * SLOT + i * 4096 + t4 * 1024)); };
    const u32 vfg = WS_FG + cl * FL;
    cn_await(CN_H, 4);
    STAMP(7);

    {
        if (w >= 4) {
            poll_flags(rsW, vfg, 1, ep, lane);
        }
        f4 acc[2][4];
#pragma unroll
        for (int ft = 0; ft < 2; ++ft) {
            const f4 bv = *(LAS f4*)(size_t)(L_B1 + (32 * w + 16 * ft + 4 * g4) * 4);
#pragma unroll
            for (int t = 0; t < 4; ++t) acc[ft][t] = bv;
        }
        v2u fg = {0u, 0u};
        if (w < 4) { fg = ld8c<0>(rsW, lane == 0 ? vfg : OOB, 0); __builtin_amdgcn_s_setprio(1); }
        const u32 ab = L_HN + lane * 16;
        bf8 Bq[3][4];
#pragma unroll
        for (int s = 0; s < 2; ++s)
#pragma unroll
            for (int t = 0; t < 4; ++t) Bq[s][t] = lds128(ab + (t * 14 + s) * 1024);
        SB();
        auto mloop = [&](auto second) {
#pragma unroll
            for (int s = 0; s < 14; ++s) {
#pragma unroll
                for (int t = 0; t < 4; ++t) {
                    if (s + 2 < 14) Bq[(s + 2) % 3][t] = lds128(ab + (t * 14 + s + 2) * 1024);
                    acc[0][t] = mfma16(A1[s][0], Bq[s % 3][t], acc[0][t]);
                    acc[1][t] = mfma16(A1[s][1], Bq[s % 3][t], acc[1][t]);
                    SB();
                }
                if constexpr (decltype(second)::value) {
                    if (s < 10) {
                        const int n0 = 2 * s, n1 = 2 * s + 1;
                        w2_piece(n0 / 7, 2 + n0 / 7, n0 % 7); w2_piece(n1 / 7, 2 + n1 / 7, n1 % 7);
                    } else if (s == 10) w2_piece(2, 4, 6);
                    SB();
                }
            }
        };
        if (w < 4) mloop(IC<0>{}); else { mloop(IC<1>{}); WAITV(0); }
        __builtin_amdgcn_s_setprio(0);
        STAMP(8);
        cn_signal(CN_M, lane);
        if (w < 4) {
            WAITV(0); PIN1(fg);
            if (!__all(lane != 0 || (fg.x == (u32)ep && fg.y == (u32)(ep >> 32)))) poll_flags(rsW, vfg, 1, ep, lane);
        }
        const u32 vg = WS_G + (cl * 4 * 56 + j * 8 + w) * 1024 + lane * 16;
        const float gk1 = -2.0f * 0.7978845608028654f * 1.4426950408889634f * 0.044715f;
        asm volatile("s_nop 7\n\ts_nop 7" : "+v"(acc[0][0]), "+v"(acc[0][1]), "+v"(acc[0][2]), "+v"(acc[0][3]), "+v"(acc[1][0]), "+v"(acc[1][1]), "+v"(acc[1][2]), "+v"(acc[1][3]));
        v4u go[4];
#pragma unroll
        for (int t = 0; t < 4; ++t) {
            const v2u lo = gelu4_pk(acc[0][t][0], acc[0][t][1], acc[0][t][2], acc[0][t][3], gk1);
            const v2u hi = gelu4_pk(acc[1][t][0], acc[1][t][1], acc[1][t][2], acc[1][t][3], gk1);
            go[t] = nz4((v4u){lo.x, lo.y, hi.x, hi.y});
        }
        SB();
#pragma unroll
        for (int t = 0; t < 4; ++t) st16c(rsW, vg + t * (56 * 1024), go[t]);
        STAMP(27);
        if (w < 4) {
            cn_await(CN_M, 8);
            w2_run(3, 0); w2_run(4, 1);
        }
    }

    {
        u32 a2[4];
        const int row = 4 * g4 + q4;
#pragma unroll
        for (int ct = 0; ct < 4; ++ct) a2[ct] = row * 128 + (((ct ^ ((row >> 1) & 3)) << 1) | (p4 >> 1)) * 16 + (p4 & 1) * 8;
        f4 acc[4] = {};
        const u32 vg = WS_G + (cl * 4 + t4) * (56 * 1024) + lane * 16;
        v4u ga[7], gb[7], gc[7], gd[7];
        auto ldg = [&](int phi, int i) { return ld16c<0>(rsW, vg, (u32)(phi * 1024 + prod(i) * 8192)); };
        auto quarter = [&](v4u (&gfr)[7], int phi, int slot, auto wait, auto hook) {
            u32 a[4];
#pragma unroll
            for (int ct = 0; ct < 4; ++ct) a[ct] = a2[ct] + (u32)slot * SLOT;
            bf8 Aw[3][4];
#pragma unroll
            for (int p = 0; p < 2; ++p)
#pragma unroll
                for (int ct = 0; ct < 4; ++ct) Aw[p][ct] = trfrag(a[ct] + p * 4096, a[ct] + p * 4096 + 2048);
            SB();
            auto step = [&](auto pc) {
                constexpr int p = decltype(pc)::value;
                wait(pc);
                PIN1(gfr[p]);
                if (has_empty(mx4(0u, gfr[p]))) refetch1(gfr[p], rsW, vg, (u32)(phi * 1024 + prod(p) * 8192));
                SB();
#pragma unroll
                for (int ct = 0; ct < 4; ++ct) {
                    if (p + 2 < 7) Aw[(p + 2) % 3][ct] = trfrag(a[ct] + (p + 2) * 4096, a[ct] + (p + 2) * 4096 + 2048);
                    acc[ct] = mfma16(Aw[p % 3][ct], asbf(gfr[p]), acc[ct]);
                    SB();
                }
                hook(pc);
                SB();
            };
            step(IC<0>{}); step(IC<1>{}); step(IC<2>{}); step(IC<3>{}); step(IC<4>{}); step(IC<5>{}); step(IC<6>{});
        };
        if (wh) __builtin_amdgcn_s_sleep(SLEEP_GB); else __builtin_amdgcn_s_sleep(SLEEP_GA);
        STAMP(9);
#pragma unroll
        for (int p = 0; p < 7; ++p) ga[p] = ldg(wh, p);
        STAMP(10);
        quarter(ga, wh, wh ? 3 : 2,
                [&](auto) { waitv<6>(); },
                [&](auto pc) { gb[decltype(pc)::value] = ldg(2 + wh, decltype(pc)::value); });
        BAR();
        STAMP(28);
        quarter(gb, 2 + wh, wh ? 0 : 4,
                [&](auto pc) { waitv<6 + decltype(pc)::value>(); },
                [&](auto pc) { w2_piece(wh ? 6 : 5, wh ? 3 : 2, decltype(pc)::value); gc[decltype(pc)::value] = ldg(4 + wh, decltype(pc)::value); });
        STAMP(29);
        waitv<1>();
        BAR();
        STAMP(11);
        quarter(gc, 4 + wh, wh ? 2 : 1,
                [&](auto pc) { if constexpr (decltype(pc)::value == 6) { if (wh) waitv<12>(); else waitv<6>(); } },
                [&](auto pc) { if (wh) w2_piece(7, 4, decltype(pc)::value); gd[decltype(pc)::value] = ldg(6 + wh, decltype(pc)::value); });
        waitv<1>();
        BAR();
        quarter(gd, 6 + wh, wh ? 4 : 3,
                [&](auto pc) { waitv<6 - decltype(pc)::value>(); },
                [&](auto) {});
        STAMP(12);
        f4 keep0, keep1, send0, send1;
        if (wh == 0) {
            keep0 = acc[0] + (f4){blo(hp[0].x), bhi(hp[0].x), blo(hp[0].y), bhi(hp[0].y)};
            keep1 = acc[1] + (f4){blo(hp[1].x), bhi(hp[1].x), blo(hp[1].y), bhi(hp[1].y)};
            send0 = acc[2] + (f4){blo(hp[2].x), bhi(hp[2].x), blo(hp[2].y), bhi(hp[2].y)};
            send1 = acc[3] + (f4){blo(hp[3].x), bhi(hp[3].x), blo(hp[3].y), bhi(hp[3].y)};
        } else { keep0 = acc[2]; keep1 = acc[3]; send0 = acc[0]; send1 = acc[1]; }
        *(LAS f4*)(size_t)(L_RED + w * 2048 + lane * 16) = send0;
        *(LAS f4*)(size_t)(L_RED + w * 2048 + 1024 + lane * 16) = send1;
        BAR();
        keep0 += *(LAS f4*)(size_t)(L_RED + (w ^ 4) * 2048 + lane * 16) + *(LAS f4*)(size_t)(L_B2 + (32 * wh + 4 * g4) * 4);
        keep1 += *(LAS f4*)(size_t)(L_RED + (w ^ 4) * 2048 + 1024 + lane * 16) + *(LAS f4*)(size_t)(L_B2 + (32 * wh + 16 + 4 * g4) * 4);
        const u32 a0 = pk2(keep0[0], keep0[1]), a1 = pk2(keep0[2], keep0[3]), c0 = pk2(keep1[0], keep1[1]), c1 = pk2(keep1[2], keep1[3]);
        const auto s0 = __builtin_amdgcn_permlane16_swap(a0, c0, false, false);
        const auto s1 = __builtin_amdgcn_permlane16_swap(a1, c1, false, false);
        const v4u o = {s0[0], s1[0], s0[1], s1[1]};
        *(GAS v4u*)((char*)outp + (size_t)(r0 + 16 * t4 + i16) * 896 + j * 128 + wh * 64 + (g4 & 1) * 32 + (g4 >> 1) * 16) = o;
        STAMP(13); STAMPC(31);
        STAMPS_OUT();
    }
}

extern "C" void kernel_launch(void* const* d_in, const int* in_sizes, int n_in, void* d_out, int out_size, void* d_ws, size_t ws_size,
                              hipStream_t stream) {
    (void)in_sizes; (void)n_in; (void)out_size; (void)ws_size;
    (void)hipFuncSetAttribute((const void*)fused_block, hipFuncAttributeMaxDynamicSharedMemorySize, LDS_BYTES);
    hipLaunchKernelGGL(fused_block, dim3(256), dim3(512), LDS_BYTES, stream, (const u16*)d_in[0], (const u16*)d_in[1], (const u16*)d_in[2],
                       (const u16*)d_in[4], (const u16*)d_in[5], (const u16*)d_in[6], (const u16*)d_in[7], (u16*)d_out, (char*)d_ws);
}
```
